```python
import torch

def _softmax(x, axis=-1):
    return torch.softmax(x, dim=axis)
def _gelu(x):
    return torch.nn.functional.gelu(x, approximate="tanh")
def _silu(x):
    return torch.nn.functional.silu(x)
def _sigmoid(x):
    return torch.sigmoid(x)
def _layernorm(x, axis=-1, eps=1e-5):
    m = x.mean(dim=axis, keepdim=True)
    v = x.var(dim=axis, keepdim=True, unbiased=False)
    return (x - m) / torch.sqrt(v + eps)
def _scatter_add(tgt, idx, src, axis=0):
    return tgt.clone().index_add_(axis, idx, src)
def _segment_sum(src, seg, n):
    out = torch.zeros((n,) + src.shape[1:], dtype=src.dtype)
    return out.index_add_(0, seg, src)
def _pad(x, pw):
    flat = tuple(v for pair in reversed(pw) for v in pair)
    return torch.nn.functional.pad(x, flat)

kernel_name = 'concat_heads__gqa_block_cached__sliding_block_cached__decode'

def setup_inputs():

    _w = (lambda a: (lambda t: t.to(torch.bfloat16) if t.is_floating_point() else t)(torch.as_tensor(a)))
    def _A_setup():
        B, Sq, D = 1, 1, 512
        Hq, Hkv, Sk, Dh = 32, 8, 65536, 128
        assert Hq % Hkv == 0, (Hq, Hkv)
        return {
            "x": _w((lambda *s: torch.randn(*s, dtype=torch.bfloat16) * 0.4)(B, Sq, D)),
            "k_cache": _w((lambda *s: torch.randn(*s, dtype=torch.bfloat16) * 3.2)(B, Hkv, Sk, Dh)),
            "v_cache": _w((lambda *s: torch.randn(*s, dtype=torch.bfloat16) * 0.8)(B, Hkv, Sk, Dh)),
            "wq": _w((lambda *s: torch.randn(*s, dtype=torch.bfloat16) * 3.2)(D, Hq * Dh) * (D ** -0.5)),
            "wo": _w((lambda *s: torch.randn(*s, dtype=torch.bfloat16) * 0.8)(Hq * Dh, D) * ((Hq * Dh) ** -0.5)),
        }
    _A_in = _A_setup()

    def _B_setup():
        B, Sq, D = 1, 1, 512
        Hq, Hkv, Sk, Dh = 32, 8, 65536, 128
        assert Hq % Hkv == 0, (Hq, Hkv)
        return {
            "x": _w((lambda *s: torch.randn(*s, dtype=torch.bfloat16) * 0.2)(B, Sq, D)),
            "k_cache": _w((lambda *s: torch.randn(*s, dtype=torch.bfloat16) * 3.2)(B, Hkv, Sk, Dh)),
            "v_cache": _w((lambda *s: torch.randn(*s, dtype=torch.bfloat16) * 1.6)(B, Hkv, Sk, Dh)),
            "wq": _w((lambda *s: torch.randn(*s, dtype=torch.bfloat16) * 3.2)(D, Hq * Dh) * (D ** -0.5)),
            "wo": _w((lambda *s: torch.randn(*s, dtype=torch.bfloat16) * 1.6)(Hq * Dh, D) * ((Hq * Dh) ** -0.5)),
        }
    _B_in = _B_setup()

    return {
        "A_x": _A_in["x"],
        "A_k_cache": _A_in["k_cache"],
        "A_v_cache": _A_in["v_cache"],
        "A_wq": _A_in["wq"],
        "A_wo": _A_in["wo"],
        "B_x": _B_in["x"],
        "B_k_cache": _B_in["k_cache"],
        "B_v_cache": _B_in["v_cache"],
        "B_wq": _B_in["wq"],
        "B_wo": _B_in["wo"],
    }

def reference(A_x, A_k_cache, A_v_cache, A_wq, A_wo, B_x, B_k_cache, B_v_cache, B_wq, B_wo):
    def _A_ref(x, k_cache, v_cache, wq, wo):
        B, Sq, D = x.shape
        Hq, Dh = 32, 128
        Hkv = k_cache.shape[1]
        rep = Hq // Hkv
        q = (lambda x, *d: x.permute(*d))((x @ wq).reshape(B, Sq, Hq, Dh), 0, 2, 1, 3)
        k = (lambda x, n, axis: torch.repeat_interleave(x, n, dim=axis))(k_cache, rep, axis=1)
        v = (lambda x, n, axis: torch.repeat_interleave(x, n, dim=axis))(v_cache, rep, axis=1)
        scores = torch.einsum("bhqd,bhkd->bhqk", q, k) * (Dh ** -0.5)
        p = _softmax(scores, axis=-1)
        o = torch.einsum("bhqk,bhkd->bhqd", p, v)
        return (lambda x, *d: x.permute(*d))(o, 0, 2, 1, 3).reshape(B, Sq, Hq * Dh) @ wo

    def _B_ref(x, k_cache, v_cache, wq, wo):
        B, Sq, D = x.shape
        Hq, Dh = 32, 128
        Hkv, Sk = k_cache.shape[1], k_cache.shape[2]
        W = min(4096, max(256, Sk // 4))
        rep = Hq // Hkv
        q = (lambda x, *d: x.permute(*d))((x @ wq).reshape(B, Sq, Hq, Dh), 0, 2, 1, 3)
        k = (lambda x, n, axis: torch.repeat_interleave(x, n, dim=axis))(k_cache[:, :, Sk - W:, :], rep, axis=1)
        v = (lambda x, n, axis: torch.repeat_interleave(x, n, dim=axis))(v_cache[:, :, Sk - W:, :], rep, axis=1)
        scores = torch.einsum("bhqd,bhkd->bhqk", q, k) * (Dh ** -0.5)
        p = _softmax(scores, axis=-1)
        o = torch.einsum("bhqk,bhkd->bhqd", p, v)
        return (lambda x, *d: x.permute(*d))(o, 0, 2, 1, 3).reshape(B, Sq, Hq * Dh) @ wo

    a = _A_ref(A_x, A_k_cache, A_v_cache, A_wq, A_wo)
    b = _B_ref(B_x, B_k_cache, B_v_cache, B_wq, B_wo)
    return torch.concatenate([a, b], axis=-1)
```

```cpp
#include <hip/hip_runtime.h>
#include <hip/hip_bf16.h>
#include <stdint.h>

#define MIMIC_REF 0
#define STAMPS 0

typedef unsigned u32;
typedef unsigned long long u64;
typedef u32 u32x2 __attribute__((ext_vector_type(2)));
typedef u32 u32x4 __attribute__((ext_vector_type(4)));
typedef float f32x4 __attribute__((ext_vector_type(4)));
typedef __bf16 bf16x8 __attribute__((ext_vector_type(8)));
typedef __bf16 bf16x2 __attribute__((ext_vector_type(2)));
#define GAS __attribute__((address_space(1)))
typedef GAS u64 gu64;

constexpr int D_MODEL = 512, HQ = 32, HKV = 8, SK = 65536, DH = 128, WIN = 4096;
constexpr int NWG = 256, NTHR = 256;
constexpr int TILE_KEYS = 32, TILE_BYTES = TILE_KEYS * DH * 2;
constexpr int NPROD_A = 30, NPROD_B = 2;
constexpr float C2 = 0.08838834764831845f * 1.4426950408889634f;

constexpr size_t WS_GEN = 0;
constexpr size_t WS_Q = 4096;
constexpr size_t WS_H1D = WS_Q + 2 * 4096 * 8;
constexpr size_t WS_H1M = WS_H1D + (size_t)2 * 8 * 16 * 32 * 32 * 8;
constexpr size_t WS_H2 = WS_H1M + (size_t)2 * 8 * 16 * 32 * 2 * 8;
constexpr size_t WS_END = WS_H2 + (size_t)2 * 32 * 128 * 16 * 8;
constexpr size_t WS_STAMP = 8u << 20;

#define LDS_KB 84
constexpr int LDS_BYTES = LDS_KB * 1024;
constexpr u32 SPIN_CAP = 1u << 18;

struct Params {
  const char* x[2]; const char* k[2]; const char* v[2]; const char* wq[2]; const char* wo[2];
  unsigned short* out; char* ws;
};

__device__ __forceinline__ __amdgpu_buffer_rsrc_t mkrsrc(const void* p, int bytes) {
  return __builtin_amdgcn_make_buffer_rsrc((void*)p, 0, bytes, 0x00020000);
}
__device__ __forceinline__ u32x4 ld_nt(__amdgpu_buffer_rsrc_t r, u32 off) { return __builtin_amdgcn_raw_buffer_load_b128(r, off, 0, 2); }
__device__ __forceinline__ u32x4 ld_sc1(__amdgpu_buffer_rsrc_t r, u32 off) { return __builtin_amdgcn_raw_buffer_load_b128(r, off, 0, 16); }
__device__ __forceinline__ void st_sc1(__amdgpu_buffer_rsrc_t r, u32 off, u32x4 v) { __builtin_amdgcn_raw_buffer_store_b128(v, r, off, 0, 16); }
__device__ __forceinline__ float dot2(u32 a, u32 b, float c) {
  return __builtin_amdgcn_fdot2_f32_bf16(__builtin_bit_cast(bf16x2, a), __builtin_bit_cast(bf16x2, b), c, false);
}
__device__ __forceinline__ u32 perm_lo(u32 hi, u32 lo) { return __builtin_amdgcn_perm(hi, lo, 0x05040100u); }
__device__ __forceinline__ u32 perm_hi(u32 hi, u32 lo) { return __builtin_amdgcn_perm(hi, lo, 0x07060302u); }
__device__ __forceinline__ u32 pack_bf16(float lo, float hi) {
  bf16x2 r = {(__bf16)lo, (__bf16)hi};
  return __builtin_bit_cast(u32, r);
}
template <int CTRL> __device__ __forceinline__ float dpp(float x) {
  return __builtin_bit_cast(float, __builtin_amdgcn_mov_dpp(__builtin_bit_cast(int, x), CTRL, 0xf, 0xf, true));
}
constexpr int ROR2 = 0x122, ROR4 = 0x124, ROR8 = 0x128;
__device__ __forceinline__ float f(u32 x) { return __uint_as_float(x); }
__device__ __forceinline__ u32 u(float x) { return __float_as_uint(x); }
__device__ __forceinline__ float swap32_add(float a, float b) { auto s = __builtin_amdgcn_permlane32_swap(u(a), u(b), false, false); return f(s[0]) + f(s[1]); }
__device__ __forceinline__ float swap16_add(float a, float b) { auto s = __builtin_amdgcn_permlane16_swap(u(a), u(b), false, false); return f(s[0]) + f(s[1]); }
__device__ __forceinline__ float xrow_max(float x) {
  auto s = __builtin_amdgcn_permlane16_swap(u(x), u(x), false, false); x = fmaxf(f(s[0]), f(s[1]));
  auto t = __builtin_amdgcn_permlane32_swap(u(x), u(x), false, false); return fmaxf(f(t[0]), f(t[1]));
}
__device__ __forceinline__ float xrow_sum(float x) { x = swap16_add(x, x); return swap32_add(x, x); }
__device__ __forceinline__ float ex2(float x) { return __builtin_amdgcn_exp2f(x); }
__device__ __forceinline__ void wg_barrier() { asm volatile("s_waitcnt lgkmcnt(0)\n\ts_barrier" ::: "memory"); }
__device__ __forceinline__ u32 enc24(float v, u32 tlo) { return ((u(v) + 0x80u) & 0xffffff00u) | tlo; }
__device__ __forceinline__ float dec24(u32 lo) { return f(lo & 0xffffff00u); }
#define STAMP(i) do { } while (0)

__global__ void __launch_bounds__(NTHR, 2)
concat_heads_attn_kernel(Params p) {
  extern __shared__ __attribute__((aligned(16))) char smem[];
  float* s_qred = (float*)smem;
  u32* s_q = (u32*)(smem + 512);
  float* s_ml = (float*)(smem + 2048);
  float* s_acc = (float*)(smem + 4096);
  u32* s_o = (u32*)(smem + 16384);
  float* s_fin = (float*)(smem + 16640);

  const int tid = threadIdx.x, lane = tid & 63;
  const int wave = __builtin_amdgcn_readfirstlane(tid >> 6);
  const int w = blockIdx.x, g = w & 7, i = w >> 3;
  STAMP(0);
  const int l15 = lane & 15, lq = lane >> 4;

  const int rb = i >> 4;
  const int ab = i >= NPROD_A ? 1 : 0;
  const int pidx = ab ? i - NPROD_A : i;
  const int t0 = ab ? pidx * 64 : pidx * 68 + min(pidx, 8);
  const int nt = ab ? 64 : 68 + (pidx < 8 ? 1 : 0);
  const size_t kvoff = ((size_t)g * SK + (ab ? SK - WIN : 0) + (size_t)t0 * TILE_KEYS) * (DH * 2);
  const char* kp = (ab ? p.k[1] : p.k[0]) + kvoff;
  const char* vp = (ab ? p.v[1] : p.v[0]) + kvoff;

  const __amdgpu_buffer_rsrc_t rws = mkrsrc(p.ws, (int)WS_END);
  const u64 gen = __hip_atomic_load((gu64*)(p.ws + WS_GEN), __ATOMIC_RELAXED, __HIP_MEMORY_SCOPE_AGENT);
  u32x4 xw, wq[8];
  {
    const int rho = lane >> 2, chi = lane & 3;
    const int row0 = 128 * wave + 8 * rho;
    xw = *(const GAS u32x4*)((rb ? p.x[1] : p.x[0]) + row0 * 2);
    const __amdgpu_buffer_rsrc_t rq = mkrsrc((rb ? p.wq[1] : p.wq[0]) + (size_t)(g * 512 + (i & 15) * 32) * 2, D_MODEL * HQ * DH * 2);
    const u32 off = (u32)row0 * (HQ * DH * 2) + chi * 16;
#pragma unroll
    for (int n = 0; n < 8; ++n) wq[n] = ld_nt(rq, off + n * (HQ * DH * 2));
  }
  u32x4 K[2][8], V[2][8];
  const u32 koff0 = (4 * (l15 & 3) + (l15 >> 2)) * 256 + lq * 16, koff1 = koff0 + 4096;
  const u32 voff0 = lane * 16, voff1 = voff0 + 4096;
  auto issue = [&](int r, int k) {
    const int t = wave + 4 * k;
    const int nb = t < nt ? TILE_BYTES : 0;
    const __amdgpu_buffer_rsrc_t rk = mkrsrc(kp + (size_t)t * TILE_BYTES, nb);
    const __amdgpu_buffer_rsrc_t rv = mkrsrc(vp + (size_t)t * TILE_BYTES, nb);
#pragma unroll
    for (int j = 0; j < 4; ++j) K[r][j] = ld_nt(rk, koff0 + j * 64);
#pragma unroll
    for (int j = 0; j < 4; ++j) K[r][4 + j] = ld_nt(rk, koff1 + j * 64);
#pragma unroll
    for (int j = 0; j < 4; ++j) V[r][j] = ld_nt(rv, voff0 + j * 1024);
#pragma unroll
    for (int j = 0; j < 4; ++j) V[r][4 + j] = ld_nt(rv, voff1 + j * 1024);
    __builtin_amdgcn_sched_barrier(0);
  };
  __builtin_amdgcn_sched_barrier(0);
  issue(0, 0);

  {
    float a[8];
#pragma unroll
    for (int c = 0; c < 8; ++c) a[c] = 0.f;
#pragma unroll
    for (int n = 0; n < 8; n += 2)
#pragma unroll
      for (int c = 0; c < 4; ++c) {
        a[2 * c] = dot2(perm_lo(wq[n + 1][c], wq[n][c]), xw[n >> 1], a[2 * c]);
        a[2 * c + 1] = dot2(perm_hi(wq[n + 1][c], wq[n][c]), xw[n >> 1], a[2 * c + 1]);
      }
    STAMP(1);
    float b[4], c2[2];
#pragma unroll
    for (int c = 0; c < 4; ++c) b[c] = swap32_add(a[c], a[c + 4]);
#pragma unroll
    for (int c = 0; c < 2; ++c) c2[c] = swap16_add(b[c], b[c + 2]);
#pragma unroll
    for (int c = 0; c < 2; ++c) { c2[c] += dpp<ROR8>(c2[c]); c2[c] += dpp<ROR4>(c2[c]); }
    if (l15 < 4) *(float2*)(s_qred + wave * 32 + 8 * l15 + 2 * lq) = make_float2(c2[0], c2[1]);
  }
  wg_barrier();
  const u64 tag = gen + 1;
  const u32 thi = (u32)(tag >> 8), tlo = (u32)tag & 0xffu;
  const u32 qhi = (u32)(tag >> 16), qlo = (u32)tag & 0xffffu;
  if (tid < 32) {
    const float q = (s_qred[tid] + s_qred[32 + tid]) + (s_qred[64 + tid] + s_qred[96 + tid]);
    const u32 qb = pack_bf16(q, 0.f) << 16;
    __hip_atomic_store((gu64*)(p.ws + WS_Q) + rb * 4096 + g * 512 + (i & 15) * 32 + tid, ((u64)qhi << 32) | (qb | qlo), __ATOMIC_RELAXED, __HIP_MEMORY_SCOPE_AGENT);
  }
  STAMP(2);
  const u32 qoff = (u32)WS_Q + (ab * 4096 + g * 512 + 2 * tid) * 8;
  bool timeout = false;
  asm volatile("s_waitcnt vmcnt(8)" ::: "memory");
  u32x4 qg = ld_sc1(rws, qoff);
  __builtin_amdgcn_sched_barrier(0);
  issue(1, 1);
  if (!__all(((qg[0] & 0xffffu) == qlo) & (qg[1] == qhi) & ((qg[2] & 0xffffu) == qlo) & (qg[3] == qhi))) {
    u32 spins = 0;
    while (true) {
      qg = ld_sc1(rws, qoff);
      if (__all(((qg[0] & 0xffffu) == qlo) & (qg[1] == qhi) & ((qg[2] & 0xffffu) == qlo) & (qg[3] == qhi))) break;
      if (++spins > SPIN_CAP) { timeout = true; break; }
      __builtin_amdgcn_s_sleep(1);
    }
  }
  s_q[tid] = perm_hi(qg[2], qg[0]);
  wg_barrier();
  u32x4 Q[4];
#pragma unroll
  for (int uu = 0; uu < 4; ++uu) Q[uu] = *(const u32x4*)(s_q + (l15 & 3) * 64 + uu * 16 + lq * 4);
  STAMP(3);

  float m = -INFINITY, l = 0.f;
  f32x4 acc[8];
#pragma unroll
  for (int t = 0; t < 8; ++t) acc[t] = f32x4{0.f, 0.f, 0.f, 0.f};
  auto compute = [&](int r, int k) {
    if (wave + 4 * k < nt) {
      f32x4 s0 = {0.f, 0.f, 0.f, 0.f}, s1 = {0.f, 0.f, 0.f, 0.f};
#pragma unroll
      for (int uu = 0; uu < 4; ++uu) {
        s0 = __builtin_amdgcn_mfma_f32_16x16x32_bf16(__builtin_bit_cast(bf16x8, K[r][uu]), __builtin_bit_cast(bf16x8, Q[uu]), s0, 0, 0, 0);
        s1 = __builtin_amdgcn_mfma_f32_16x16x32_bf16(__builtin_bit_cast(bf16x8, K[r][4 + uu]), __builtin_bit_cast(bf16x8, Q[uu]), s1, 0, 0, 0);
      }
      float s[8] = {s0[0], s0[1], s0[2], s0[3], s1[0], s1[1], s1[2], s1[3]};
      constexpr float CC = C2;
      float mx = fmaxf(fmaxf(fmaxf(s[0], s[1]), fmaxf(s[2], s[3])), fmaxf(fmaxf(s[4], s[5]), fmaxf(s[6], s[7])));
      mx = xrow_max(mx);
      const float mn = fmaxf(m, mx);
      if (__any(mn > m)) {
        const float alpha = ex2((m - mn) * CC);
        l *= alpha;
#pragma unroll
        for (int t = 0; t < 8; ++t) acc[t] *= alpha;
        m = mn;
      }
      const float mc = m * CC;
      float pr[8];
#pragma unroll
      for (int j = 0; j < 8; ++j) pr[j] = ex2(fmaf(s[j], CC, -mc));
      l += ((pr[0] + pr[1]) + (pr[2] + pr[3])) + ((pr[4] + pr[5]) + (pr[6] + pr[7]));
      const u32x4 P = {pack_bf16(pr[0], pr[1]), pack_bf16(pr[2], pr[3]), pack_bf16(pr[4], pr[5]), pack_bf16(pr[6], pr[7])};
#pragma unroll
      for (int t = 0; t < 8; ++t) {
        u32x4 A;
#pragma unroll
        for (int jj = 0; jj < 4; ++jj)
          A[jj] = (t & 1) ? perm_hi(V[r][2 * jj + 1][t >> 1], V[r][2 * jj][t >> 1]) : perm_lo(V[r][2 * jj + 1][t >> 1], V[r][2 * jj][t >> 1]);
        acc[t] = __builtin_amdgcn_mfma_f32_16x16x32_bf16(__builtin_bit_cast(bf16x8, A), __builtin_bit_cast(bf16x8, P), acc[t], 0, 0, 0);
      }
    }
    __builtin_amdgcn_sched_barrier(0);
  };
#pragma nounroll
  for (int k = 0; k < 16; k += 2) {
    compute(0, k); issue(0, k + 2);
    compute(1, k + 1); issue(1, k + 3);
  }
  const int hh = (i & 15) >> 2, sl = i & 3;
  u32x4 wo[8];
  {
    const __amdgpu_buffer_rsrc_t ro = mkrsrc((rb ? p.wo[1] : p.wo[0]) + (size_t)((g * 4 + hh) * DH + 32 * sl) * (D_MODEL * 2), 32 * D_MODEL * 2);
#pragma unroll
    for (int n = 0; n < 8; ++n) {
      const int jj = 8 * lq + n;
      wo[n] = ld_nt(ro, (u32)(8 * (jj & 3) + (jj >> 2)) * (D_MODEL * 2) + (128 * wave + 8 * l15) * 2);
    }
    __builtin_amdgcn_sched_barrier(0);
  }
  compute(0, 16);
  compute(1, 17);
  STAMP(4);
  constexpr float CM = C2;

  l = xrow_sum(l);
  {
    const int h = l15 & 3, kq = l15 >> 2;
    float* dst = s_acc + (wave * 4 + h) * 128 + 32 * lq + 8 * kq;
    f32x4 a0 = kq == 0 ? acc[0] : kq == 1 ? acc[2] : kq == 2 ? acc[4] : acc[6];
    f32x4 a1 = kq == 0 ? acc[1] : kq == 1 ? acc[3] : kq == 2 ? acc[5] : acc[7];
    *(f32x4*)dst = a0; *(f32x4*)(dst + 4) = a1;
    if (lane < 4) *(float2*)(s_ml + (wave * 4 + lane) * 2) = make_float2(m * CM, l);
  }
  wg_barrier();
  {
    const int h = wave;
    float mw[4], lw[4];
#pragma unroll
    for (int ww = 0; ww < 4; ++ww) { const float2 t = *(const float2*)(s_ml + (ww * 4 + h) * 2); mw[ww] = t.x; lw[ww] = t.y; }
    const float M = dec24(enc24(fmaxf(fmaxf(mw[0], mw[1]), fmaxf(mw[2], mw[3])), 0));
    float o0 = 0.f, o1 = 0.f, L = 0.f;
#pragma unroll
    for (int ww = 0; ww < 4; ++ww) {
      const float fw = ex2(mw[ww] - M);
      const float2 a = *(const float2*)(s_acc + (ww * 4 + h) * 128 + 2 * lane);
      o0 = fmaf(fw, a.x, o0); o1 = fmaf(fw, a.y, o1); L = fmaf(fw, lw[ww], L);
    }
    const int role = (ab * 8 + g) * 16 + h * 4 + (lane >> 4);
    st_sc1(rws, (u32)WS_H1D + ((role * 32 + pidx) * 32 + 2 * l15) * 8, u32x4{enc24(o0, tlo), thi, enc24(o1, tlo), thi});
    if (l15 == 0) st_sc1(rws, (u32)WS_H1M + (role * 32 + pidx) * 16, u32x4{enc24(M, tlo), thi, enc24(L, tlo), thi});
  }
  STAMP(5);

  {
    const int np = rb ? NPROD_B : NPROD_A;
    const int role = (rb * 8 + g) * 16 + (i & 15);
    const int pp = lane >> 1, half = lane & 1;
    const bool live = pp < np;
    const u32 od = (u32)WS_H1D + ((role * 32 + pp) * 32 + 8 * wave + 4 * half) * 8;
    const u32 om = (u32)WS_H1M + (role * 32 + pp) * 16;
    u32x4 d0, d1, ml;
    u32 spins = 0;
    while (true) {
      d0 = ld_sc1(rws, od); d1 = ld_sc1(rws, od + 16); ml = ld_sc1(rws, om);
      const u32 bad = ((d0[0] ^ tlo) & 0xffu) | (d0[1] ^ thi) | ((d0[2] ^ tlo) & 0xffu) | (d0[3] ^ thi) |
                      ((d1[0] ^ tlo) & 0xffu) | (d1[1] ^ thi) | ((d1[2] ^ tlo) & 0xffu) | (d1[3] ^ thi) |
                      ((ml[0] ^ tlo) & 0xffu) | (ml[1] ^ thi) | ((ml[2] ^ tlo) & 0xffu) | (ml[3] ^ thi);
      if (__all(bad == 0 || !live)) break;
      if (timeout || ++spins > SPIN_CAP) { timeout = true; break; }
      __builtin_amdgcn_s_sleep(1);
    }
    STAMP(6);
    const float mp = live ? dec24(ml[0]) : -INFINITY;
    float M = mp;
    M = fmaxf(M, dpp<ROR2>(M)); M = fmaxf(M, dpp<ROR4>(M)); M = fmaxf(M, dpp<ROR8>(M)); M = xrow_max(M);
    const float fw = live ? ex2(mp - M) : 0.f;
    float L = live ? fw * dec24(ml[2]) : 0.f;
    float v0 = live ? fw * dec24(d0[0]) : 0.f, v1 = live ? fw * dec24(d0[2]) : 0.f, v2 = live ? fw * dec24(d1[0]) : 0.f, v3 = live ? fw * dec24(d1[2]) : 0.f;
    L += dpp<ROR2>(L); L += dpp<ROR4>(L); L += dpp<ROR8>(L); L = xrow_sum(L);
    float y0 = swap32_add(v0, v2), y1 = swap32_add(v1, v3);
    float z = swap16_add(y0, y1);
    z += dpp<ROR2>(z); z += dpp<ROR4>(z); z += dpp<ROR8>(z);
    z = z / L;
    if (l15 < 2) ((unsigned short*)s_o)[8 * wave + 4 * l15 + lq] = (unsigned short)(pack_bf16(z, 0.f) & 0xffffu);
  }
  wg_barrier();
  {
    const u32x4 op = *(const u32x4*)(s_o + 4 * lq);
    float a[8];
#pragma unroll
    for (int c = 0; c < 8; ++c) a[c] = 0.f;
#pragma unroll
    for (int n = 0; n < 8; n += 2)
#pragma unroll
      for (int c = 0; c < 4; ++c) {
        a[2 * c] = dot2(perm_lo(wo[n + 1][c], wo[n][c]), op[n >> 1], a[2 * c]);
        a[2 * c + 1] = dot2(perm_hi(wo[n + 1][c], wo[n][c]), op[n >> 1], a[2 * c + 1]);
      }
    float b[4];
#pragma unroll
    for (int c = 0; c < 4; ++c) b[c] = swap32_add(a[c], a[c + 4]);
    const float y0 = swap16_add(b[0], b[2]), y1 = swap16_add(b[1], b[3]);
    const int col = 128 * wave + 8 * l15 + 2 * lq;
    const int prod = g * 16 + (i & 15);
    st_sc1(rws, (u32)WS_H2 + (((rb * 32 + (col >> 4)) * 128 + prod) * 16 + (col & 15)) * 8, u32x4{enc24(y0, tlo), thi, enc24(y1, tlo), thi});
  }
  STAMP(7);

  if (w < 64) {
    const int fb = w >> 5, fc = w & 31;
    const int cc = tid & 7, pp = tid >> 3;
    const u32 o2 = (u32)WS_H2 + (((fb * 32 + fc) * 128 + pp) * 16 + 2 * cc) * 8;
    u32x4 d[4];
    u32 spins = 0;
    while (true) {
      u32 bad = 0;
#pragma unroll
      for (int k = 0; k < 4; ++k) d[k] = ld_sc1(rws, o2 + k * (32 * 128));
#pragma unroll
      for (int k = 0; k < 4; ++k) bad |= ((d[k][0] ^ tlo) & 0xffu) | (d[k][1] ^ thi) | ((d[k][2] ^ tlo) & 0xffu) | (d[k][3] ^ thi);
      if (__all(bad == 0)) break;
      if (timeout || ++spins > SPIN_CAP) { timeout = true; break; }
      __builtin_amdgcn_s_sleep(1);
    }
    STAMP(8);
    float y0 = (dec24(d[0][0]) + dec24(d[1][0])) + (dec24(d[2][0]) + dec24(d[3][0]));
    float y1 = (dec24(d[0][2]) + dec24(d[1][2])) + (dec24(d[2][2]) + dec24(d[3][2]));
    float z = swap32_add(y0, y1);
    z = swap16_add(z, z);
    z += dpp<ROR8>(z);
    if ((lane & 31) < 8) s_fin[wave * 16 + 2 * cc + (lane >> 5)] = z;
    wg_barrier();
    if (tid < 8) {
      const float r0 = (s_fin[2 * tid] + s_fin[16 + 2 * tid]) + (s_fin[32 + 2 * tid] + s_fin[48 + 2 * tid]);
      const float r1 = (s_fin[2 * tid + 1] + s_fin[16 + 2 * tid + 1]) + (s_fin[32 + 2 * tid + 1] + s_fin[48 + 2 * tid + 1]);
      u32 o = pack_bf16(r0, r1);
      if (timeout) o = 0x7fc07fc0u;
      *(GAS u32*)((char*)p.out + (fb * 512 + fc * 16 + 2 * tid) * 2) = o;
    }
    if (w == 0 && wave == 0) {
      u32 sp = 0;
      while (true) {
        const u32x4 e0 = ld_sc1(rws, (u32)WS_H2 + ((32 * 128 + lane) * 16) * 8), e1 = ld_sc1(rws, (u32)WS_H2 + ((32 * 128 + 64 + lane) * 16) * 8);
        const u32 bad = ((e0[0] ^ tlo) & 0xffu) | (e0[1] ^ thi) | ((e1[0] ^ tlo) & 0xffu) | (e1[1] ^ thi);
        if (__all(bad == 0)) break;
        if (timeout || ++sp > SPIN_CAP) break;
        __builtin_amdgcn_s_sleep(1);
      }
      if (lane == 0) __hip_atomic_store((gu64*)(p.ws + WS_GEN), tag, __ATOMIC_RELAXED, __HIP_MEMORY_SCOPE_AGENT);
    }
  }
  STAMP(9);
}

extern "C" void kernel_launch(void* const* d_in, const int* in_sizes, int n_in, void* d_out, int out_size, void* d_ws, size_t ws_size,
                              hipStream_t stream) {
  (void)in_sizes; (void)n_in; (void)out_size; (void)ws_size;
  Params p;
  for (int b = 0; b < 2; ++b) {
    p.x[b] = (const char*)d_in[5 * b + 0]; p.k[b] = (const char*)d_in[5 * b + 1]; p.v[b] = (const char*)d_in[5 * b + 2];
    p.wq[b] = (const char*)d_in[5 * b + 3]; p.wo[b] = (const char*)d_in[5 * b + 4];
  }
  p.out = (unsigned short*)d_out; p.ws = (char*)d_ws;
  (void)hipFuncSetAttribute((const void*)concat_heads_attn_kernel, hipFuncAttributeMaxDynamicSharedMemorySize, LDS_BYTES);
  hipLaunchKernelGGL(concat_heads_attn_kernel, dim3(NWG), dim3(NTHR), LDS_BYTES, stream, p);
}
```

```cpp
#include <hip/hip_runtime.h>
#include <hip/hip_bf16.h>
#include <stdint.h>

#define MIMIC_REF 1
#define STAMPS 0

typedef unsigned u32;
typedef unsigned long long u64;
typedef u32 u32x2 __attribute__((ext_vector_type(2)));
typedef u32 u32x4 __attribute__((ext_vector_type(4)));
typedef float f32x4 __attribute__((ext_vector_type(4)));
typedef __bf16 bf16x8 __attribute__((ext_vector_type(8)));
typedef __bf16 bf16x2 __attribute__((ext_vector_type(2)));
#define GAS __attribute__((address_space(1)))
typedef GAS u64 gu64;

constexpr int D_MODEL = 512, HQ = 32, HKV = 8, SK = 65536, DH = 128, WIN = 4096;
constexpr int NWG = 256, NTHR = 256;
constexpr int TILE_KEYS = 32, TILE_BYTES = TILE_KEYS * DH * 2;
constexpr int NPROD_A = 30, NPROD_B = 2;
constexpr float C2 = 0.08838834764831845f * 1.4426950408889634f;

constexpr size_t WS_GEN = 0;
constexpr size_t WS_Q = 4096;
constexpr size_t WS_H1D = WS_Q + 2 * 4096 * 8;
constexpr size_t WS_H1M = WS_H1D + (size_t)2 * 8 * 16 * 32 * 32 * 8;
constexpr size_t WS_H2 = WS_H1M + (size_t)2 * 8 * 16 * 32 * 2 * 8;
constexpr size_t WS_END = WS_H2 + (size_t)2 * 32 * 128 * 16 * 8;
constexpr size_t WS_STAMP = 8u << 20;

#define LDS_KB 84
constexpr int LDS_BYTES = LDS_KB * 1024;
constexpr u32 SPIN_CAP = 1u << 18;

struct Params {
  const char* x[2]; const char* k[2]; const char* v[2]; const char* wq[2]; const char* wo[2];
  unsigned short* out; char* ws;
};

__device__ __forceinline__ __amdgpu_buffer_rsrc_t mkrsrc(const void* p, int bytes) {
  return __builtin_amdgcn_make_buffer_rsrc((void*)p, 0, bytes, 0x00020000);
}
__device__ __forceinline__ u32x4 ld_nt(__amdgpu_buffer_rsrc_t r, u32 off) { return __builtin_amdgcn_raw_buffer_load_b128(r, off, 0, 2); }
__device__ __forceinline__ u32x4 ld_sc1(__amdgpu_buffer_rsrc_t r, u32 off) { return __builtin_amdgcn_raw_buffer_load_b128(r, off, 0, 16); }
__device__ __forceinline__ void st_sc1(__amdgpu_buffer_rsrc_t r, u32 off, u32x4 v) { __builtin_amdgcn_raw_buffer_store_b128(v, r, off, 0, 16); }
__device__ __forceinline__ float dot2(u32 a, u32 b, float c) {
  return __builtin_amdgcn_fdot2_f32_bf16(__builtin_bit_cast(bf16x2, a), __builtin_bit_cast(bf16x2, b), c, false);
}
__device__ __forceinline__ u32 perm_lo(u32 hi, u32 lo) { return __builtin_amdgcn_perm(hi, lo, 0x05040100u); }
__device__ __forceinline__ u32 perm_hi(u32 hi, u32 lo) { return __builtin_amdgcn_perm(hi, lo, 0x07060302u); }
__device__ __forceinline__ u32 pack_bf16(float lo, float hi) {
  bf16x2 r = {(__bf16)lo, (__bf16)hi};
  return __builtin_bit_cast(u32, r);
}
template <int CTRL> __device__ __forceinline__ float dpp(float x) {
  return __builtin_bit_cast(float, __builtin_amdgcn_mov_dpp(__builtin_bit_cast(int, x), CTRL, 0xf, 0xf, true));
}
constexpr int ROR2 = 0x122, ROR4 = 0x124, ROR8 = 0x128;
__device__ __forceinline__ float f(u32 x) { return __uint_as_float(x); }
__device__ __forceinline__ u32 u(float x) { return __float_as_uint(x); }
__device__ __forceinline__ float swap32_add(float a, float b) { auto s = __builtin_amdgcn_permlane32_swap(u(a), u(b), false, false); return f(s[0]) + f(s[1]); }
__device__ __forceinline__ float swap16_add(float a, float b) { auto s = __builtin_amdgcn_permlane16_swap(u(a), u(b), false, false); return f(s[0]) + f(s[1]); }
__device__ __forceinline__ float xrow_max(float x) {
  auto s = __builtin_amdgcn_permlane16_swap(u(x), u(x), false, false); x = fmaxf(f(s[0]), f(s[1]));
  auto t = __builtin_amdgcn_permlane32_swap(u(x), u(x), false, false); return fmaxf(f(t[0]), f(t[1]));
}
__device__ __forceinline__ float xrow_sum(float x) { x = swap16_add(x, x); return swap32_add(x, x); }
__device__ __forceinline__ float ex2(float x) { return __builtin_amdgcn_exp2f(x); }
__device__ __forceinline__ void wg_barrier() { asm volatile("s_waitcnt lgkmcnt(0)\n\ts_barrier" ::: "memory"); }
__device__ __forceinline__ u32 enc24(float v, u32 tlo) { return ((u(v) + 0x80u) & 0xffffff00u) | tlo; }
__device__ __forceinline__ float dec24(u32 lo) { return f(lo & 0xffffff00u); }
#define STAMP(i) do { } while (0)

__global__ void __launch_bounds__(NTHR, 2)
concat_heads_attn_kernel(Params p) {
  extern __shared__ __attribute__((aligned(16))) char smem[];
  float* s_qred = (float*)smem;
  u32* s_q = (u32*)(smem + 512);
  float* s_ml = (float*)(smem + 2048);
  float* s_acc = (float*)(smem + 4096);
  u32* s_o = (u32*)(smem + 16384);
  float* s_fin = (float*)(smem + 16640);

  const int tid = threadIdx.x, lane = tid & 63;
  const int wave = __builtin_amdgcn_readfirstlane(tid >> 6);
  const int w = blockIdx.x, g = w & 7, i = w >> 3;
  STAMP(0);
  const int l15 = lane & 15, lq = lane >> 4;

  const int rb = i >> 4;
  const int ab = i >= NPROD_A ? 1 : 0;
  const int pidx = ab ? i - NPROD_A : i;
  const int t0 = ab ? pidx * 64 : pidx * 68 + min(pidx, 8);
  const int nt = ab ? 64 : 68 + (pidx < 8 ? 1 : 0);
  const size_t kvoff = ((size_t)g * SK + (ab ? SK - WIN : 0) + (size_t)t0 * TILE_KEYS) * (DH * 2);
  const char* kp = (ab ? p.k[1] : p.k[0]) + kvoff;
  const char* vp = (ab ? p.v[1] : p.v[0]) + kvoff;

  const __amdgpu_buffer_rsrc_t rws = mkrsrc(p.ws, (int)WS_END);
  const u64 gen = __hip_atomic_load((gu64*)(p.ws + WS_GEN), __ATOMIC_RELAXED, __HIP_MEMORY_SCOPE_AGENT);
  u32x4 xw, wq[8];
  {
    const int rho = lane >> 2, chi = lane & 3;
    const int row0 = 128 * wave + 8 * rho;
    xw = *(const GAS u32x4*)((rb ? p.x[1] : p.x[0]) + row0 * 2);
    const __amdgpu_buffer_rsrc_t rq = mkrsrc((rb ? p.wq[1] : p.wq[0]) + (size_t)(g * 512 + (i & 15) * 32) * 2, D_MODEL * HQ * DH * 2);
    const u32 off = (u32)row0 * (HQ * DH * 2) + chi * 16;
#pragma unroll
    for (int n = 0; n < 8; ++n) wq[n] = ld_nt(rq, off + n * (HQ * DH * 2));
  }
  u32x4 K[2][8], V[2][8];
  const u32 koff0 = (4 * (l15 & 3) + (l15 >> 2)) * 256 + lq * 16, koff1 = koff0 + 4096;
  const u32 voff0 = lane * 16, voff1 = voff0 + 4096;
  auto issue = [&](int r, int k) {
    const int t = wave + 4 * k;
    const int nb = t < nt ? TILE_BYTES : 0;
    const __amdgpu_buffer_rsrc_t rk = mkrsrc(kp + (size_t)t * TILE_BYTES, nb);
    const __amdgpu_buffer_rsrc_t rv = mkrsrc(vp + (size_t)t * TILE_BYTES, nb);
#pragma unroll
    for (int j = 0; j < 4; ++j) K[r][j] = ld_nt(rk, koff0 + j * 64);
#pragma unroll
    for (int j = 0; j < 4; ++j) K[r][4 + j] = ld_nt(rk, koff1 + j * 64);
#pragma unroll
    for (int j = 0; j < 4; ++j) V[r][j] = ld_nt(rv, voff0 + j * 1024);
#pragma unroll
    for (int j = 0; j < 4; ++j) V[r][4 + j] = ld_nt(rv, voff1 + j * 1024);
    __builtin_amdgcn_sched_barrier(0);
  };
  __builtin_amdgcn_sched_barrier(0);
  issue(0, 0);

  {
    float a[8];
#pragma unroll
    for (int c = 0; c < 8; ++c) a[c] = 0.f;
#pragma unroll
    for (int n = 0; n < 8; n += 2)
#pragma unroll
      for (int c = 0; c < 4; ++c) {
        a[2 * c] = dot2(perm_lo(wq[n + 1][c], wq[n][c]), xw[n >> 1], a[2 * c]);
        a[2 * c + 1] = dot2(perm_hi(wq[n + 1][c], wq[n][c]), xw[n >> 1], a[2 * c + 1]);
      }
    STAMP(1);
    float b[4], c2[2];
#pragma unroll
    for (int c = 0; c < 4; ++c) b[c] = swap32_add(a[c], a[c + 4]);
#pragma unroll
    for (int c = 0; c < 2; ++c) c2[c] = swap16_add(b[c], b[c + 2]);
#pragma unroll
    for (int c = 0; c < 2; ++c) { c2[c] += dpp<ROR8>(c2[c]); c2[c] += dpp<ROR4>(c2[c]); }
    if (l15 < 4) *(float2*)(s_qred + wave * 32 + 8 * l15 + 2 * lq) = make_float2(c2[0], c2[1]);
  }
  wg_barrier();
  const u64 tag = gen + 1;
  const u32 thi = (u32)(tag >> 8), tlo = (u32)tag & 0xffu;
  const u32 qhi = (u32)(tag >> 16), qlo = (u32)tag & 0xffffu;
  if (tid < 32) {
    const float q = (s_qred[tid] + s_qred[32 + tid]) + (s_qred[64 + tid] + s_qred[96 + tid]);
    const u32 qb = pack_bf16(q, 0.f) << 16;
    __hip_atomic_store((gu64*)(p.ws + WS_Q) + rb * 4096 + g * 512 + (i & 15) * 32 + tid, ((u64)qhi << 32) | (qb | qlo), __ATOMIC_RELAXED, __HIP_MEMORY_SCOPE_AGENT);
  }
  STAMP(2);
  const u32 qoff = (u32)WS_Q + (ab * 4096 + g * 512 + 2 * tid) * 8;
  bool timeout = false;
  asm volatile("s_waitcnt vmcnt(8)" ::: "memory");
  u32x4 qg = ld_sc1(rws, qoff);
  __builtin_amdgcn_sched_barrier(0);
  issue(1, 1);
  if (!__all(((qg[0] & 0xffffu) == qlo) & (qg[1] == qhi) & ((qg[2] & 0xffffu) == qlo) & (qg[3] == qhi))) {
    u32 spins = 0;
    while (true) {
      qg = ld_sc1(rws, qoff);
      if (__all(((qg[0] & 0xffffu) == qlo) & (qg[1] == qhi) & ((qg[2] & 0xffffu) == qlo) & (qg[3] == qhi))) break;
      if (++spins > SPIN_CAP) { timeout = true; break; }
      __builtin_amdgcn_s_sleep(1);
    }
  }
  s_q[tid] = perm_hi(qg[2], qg[0]);
  wg_barrier();
  u32x4 Q[4];
#pragma unroll
  for (int uu = 0; uu < 4; ++uu) Q[uu] = *(const u32x4*)(s_q + (l15 & 3) * 64 + uu * 16 + lq * 4);
  STAMP(3);

  float m = -INFINITY, l = 0.f;
  f32x4 acc[8];
#pragma unroll
  for (int t = 0; t < 8; ++t) acc[t] = f32x4{0.f, 0.f, 0.f, 0.f};
  auto compute = [&](int r, int k) {
    if (wave + 4 * k < nt) {
      f32x4 s0 = {0.f, 0.f, 0.f, 0.f}, s1 = {0.f, 0.f, 0.f, 0.f};
#pragma unroll
      for (int uu = 0; uu < 4; ++uu) {
        s0 = __builtin_amdgcn_mfma_f32_16x16x32_bf16(__builtin_bit_cast(bf16x8, K[r][uu]), __builtin_bit_cast(bf16x8, Q[uu]), s0, 0, 0, 0);
        s1 = __builtin_amdgcn_mfma_f32_16x16x32_bf16(__builtin_bit_cast(bf16x8, K[r][4 + uu]), __builtin_bit_cast(bf16x8, Q[uu]), s1, 0, 0, 0);
      }
      float s[8] = {s0[0], s0[1], s0[2], s0[3], s1[0], s1[1], s1[2], s1[3]};
#pragma unroll
      for (int j = 0; j < 8; j += 2) {
        u32 a = pack_bf16(s[j], s[j + 1]);
        float x0 = f(a << 16) * 0.08838834764831845f, x1 = f(a & 0xffff0000u) * 0.08838834764831845f;
        a = pack_bf16(x0, x1);
        s[j] = f(a << 16); s[j + 1] = f(a & 0xffff0000u);
      }
      constexpr float CC = 1.4426950408889634f;
      float mx = fmaxf(fmaxf(fmaxf(s[0], s[1]), fmaxf(s[2], s[3])), fmaxf(fmaxf(s[4], s[5]), fmaxf(s[6], s[7])));
      mx = xrow_max(mx);
      const float mn = fmaxf(m, mx);
      if (__any(mn > m)) {
        const float alpha = ex2((m - mn) * CC);
        l *= alpha;
#pragma unroll
        for (int t = 0; t < 8; ++t) acc[t] *= alpha;
        m = mn;
      }
      const float mc = m * CC;
      float pr[8];
#pragma unroll
      for (int j = 0; j < 8; ++j) pr[j] = ex2(fmaf(s[j], CC, -mc));
      l += ((pr[0] + pr[1]) + (pr[2] + pr[3])) + ((pr[4] + pr[5]) + (pr[6] + pr[7]));
      const u32x4 P = {pack_bf16(pr[0], pr[1]), pack_bf16(pr[2], pr[3]), pack_bf16(pr[4], pr[5]), pack_bf16(pr[6], pr[7])};
#pragma unroll
      for (int t = 0; t < 8; ++t) {
        u32x4 A;
#pragma unroll
        for (int jj = 0; jj < 4; ++jj)
          A[jj] = (t & 1) ? perm_hi(V[r][2 * jj + 1][t >> 1], V[r][2 * jj][t >> 1]) : perm_lo(V[r][2 * jj + 1][t >> 1], V[r][2 * jj][t >> 1]);
        acc[t] = __builtin_amdgcn_mfma_f32_16x16x32_bf16(__builtin_bit_cast(bf16x8, A), __builtin_bit_cast(bf16x8, P), acc[t], 0, 0, 0);
      }
    }
    __builtin_amdgcn_sched_barrier(0);
  };
#pragma nounroll
  for (int k = 0; k < 16; k += 2) {
    compute(0, k); issue(0, k + 2);
    compute(1, k + 1); issue(1, k + 3);
  }
  const int hh = (i & 15) >> 2, sl = i & 3;
  u32x4 wo[8];
  {
    const __amdgpu_buffer_rsrc_t ro = mkrsrc((rb ? p.wo[1] : p.wo[0]) + (size_t)((g * 4 + hh) * DH + 32 * sl) * (D_MODEL * 2), 32 * D_MODEL * 2);
#pragma unroll
    for (int n = 0; n < 8; ++n) {
      const int jj = 8 * lq + n;
      wo[n] = ld_nt(ro, (u32)(8 * (jj & 3) + (jj >> 2)) * (D_MODEL * 2) + (128 * wave + 8 * l15) * 2);
    }
    __builtin_amdgcn_sched_barrier(0);
  }
  compute(0, 16);
  compute(1, 17);
  STAMP(4);
  constexpr float CM = 1.4426950408889634f;

  l = xrow_sum(l);
  {
    const int h = l15 & 3, kq = l15 >> 2;
    float* dst = s_acc + (wave * 4 + h) * 128 + 32 * lq + 8 * kq;
    f32x4 a0 = kq == 0 ? acc[0] : kq == 1 ? acc[2] : kq == 2 ? acc[4] : acc[6];
    f32x4 a1 = kq == 0 ? acc[1] : kq == 1 ? acc[3] : kq == 2 ? acc[5] : acc[7];
    *(f32x4*)dst = a0; *(f32x4*)(dst + 4) = a1;
    if (lane < 4) *(float2*)(s_ml + (wave * 4 + lane) * 2) = make_float2(m * CM, l);
  }
  wg_barrier();
  {
    const int h = wave;
    float mw[4], lw[4];
#pragma unroll
    for (int ww = 0; ww < 4; ++ww) { const float2 t = *(const float2*)(s_ml + (ww * 4 + h) * 2); mw[ww] = t.x; lw[ww] = t.y; }
    const float M = dec24(enc24(fmaxf(fmaxf(mw[0], mw[1]), fmaxf(mw[2], mw[3])), 0));
    float o0 = 0.f, o1 = 0.f, L = 0.f;
#pragma unroll
    for (int ww = 0; ww < 4; ++ww) {
      const float fw = ex2(mw[ww] - M);
      const float2 a = *(const float2*)(s_acc + (ww * 4 + h) * 128 + 2 * lane);
      o0 = fmaf(fw, a.x, o0); o1 = fmaf(fw, a.y, o1); L = fmaf(fw, lw[ww], L);
    }
    const int role = (ab * 8 + g) * 16 + h * 4 + (lane >> 4);
    st_sc1(rws, (u32)WS_H1D + ((role * 32 + pidx) * 32 + 2 * l15) * 8, u32x4{enc24(o0, tlo), thi, enc24(o1, tlo), thi});
    if (l15 == 0) st_sc1(rws, (u32)WS_H1M + (role * 32 + pidx) * 16, u32x4{enc24(M, tlo), thi, enc24(L, tlo), thi});
  }
  STAMP(5);

  {
    const int np = rb ? NPROD_B : NPROD_A;
    const int role = (rb * 8 + g) * 16 + (i & 15);
    const int pp = lane >> 1, half = lane & 1;
    const bool live = pp < np;
    const u32 od = (u32)WS_H1D + ((role * 32 + pp) * 32 + 8 * wave + 4 * half) * 8;
    const u32 om = (u32)WS_H1M + (role * 32 + pp) * 16;
    u32x4 d0, d1, ml;
    u32 spins = 0;
    while (true) {
      d0 = ld_sc1(rws, od); d1 = ld_sc1(rws, od + 16); ml = ld_sc1(rws, om);
      const u32 bad = ((d0[0] ^ tlo) & 0xffu) | (d0[1] ^ thi) | ((d0[2] ^ tlo) & 0xffu) | (d0[3] ^ thi) |
                      ((d1[0] ^ tlo) & 0xffu) | (d1[1] ^ thi) | ((d1[2] ^ tlo) & 0xffu) | (d1[3] ^ thi) |
                      ((ml[0] ^ tlo) & 0xffu) | (ml[1] ^ thi) | ((ml[2] ^ tlo) & 0xffu) | (ml[3] ^ thi);
      if (__all(bad == 0 || !live)) break;
      if (timeout || ++spins > SPIN_CAP) { timeout = true; break; }
      __builtin_amdgcn_s_sleep(1);
    }
    STAMP(6);
    const float mp = live ? dec24(ml[0]) : -INFINITY;
    float M = mp;
    M = fmaxf(M, dpp<ROR2>(M)); M = fmaxf(M, dpp<ROR4>(M)); M = fmaxf(M, dpp<ROR8>(M)); M = xrow_max(M);
    const float fw = live ? ex2(mp - M) : 0.f;
    float L = live ? fw * dec24(ml[2]) : 0.f;
    float v0 = live ? fw * dec24(d0[0]) : 0.f, v1 = live ? fw * dec24(d0[2]) : 0.f, v2 = live ? fw * dec24(d1[0]) : 0.f, v3 = live ? fw * dec24(d1[2]) : 0.f;
    L += dpp<ROR2>(L); L += dpp<ROR4>(L); L += dpp<ROR8>(L); L = xrow_sum(L);
    float y0 = swap32_add(v0, v2), y1 = swap32_add(v1, v3);
    float z = swap16_add(y0, y1);
    z += dpp<ROR2>(z); z += dpp<ROR4>(z); z += dpp<ROR8>(z);
    z = z / L;
    if (l15 < 2) ((unsigned short*)s_o)[8 * wave + 4 * l15 + lq] = (unsigned short)(pack_bf16(z, 0.f) & 0xffffu);
  }
  wg_barrier();
  {
    const u32x4 op = *(const u32x4*)(s_o + 4 * lq);
    float a[8];
#pragma unroll
    for (int c = 0; c < 8; ++c) a[c] = 0.f;
#pragma unroll
    for (int n = 0; n < 8; n += 2)
#pragma unroll
      for (int c = 0; c < 4; ++c) {
        a[2 * c] = dot2(perm_lo(wo[n + 1][c], wo[n][c]), op[n >> 1], a[2 * c]);
        a[2 * c + 1] = dot2(perm_hi(wo[n + 1][c], wo[n][c]), op[n >> 1], a[2 * c + 1]);
      }
    float b[4];
#pragma unroll
    for (int c = 0; c < 4; ++c) b[c] = swap32_add(a[c], a[c + 4]);
    const float y0 = swap16_add(b[0], b[2]), y1 = swap16_add(b[1], b[3]);
    const int col = 128 * wave + 8 * l15 + 2 * lq;
    const int prod = g * 16 + (i & 15);
    st_sc1(rws, (u32)WS_H2 + (((rb * 32 + (col >> 4)) * 128 + prod) * 16 + (col & 15)) * 8, u32x4{enc24(y0, tlo), thi, enc24(y1, tlo), thi});
  }
  STAMP(7);

  if (w < 64) {
    const int fb = w >> 5, fc = w & 31;
    const int cc = tid & 7, pp = tid >> 3;
    const u32 o2 = (u32)WS_H2 + (((fb * 32 + fc) * 128 + pp) * 16 + 2 * cc) * 8;
    u32x4 d[4];
    u32 spins = 0;
    while (true) {
      u32 bad = 0;
#pragma unroll
      for (int k = 0; k < 4; ++k) d[k] = ld_sc1(rws, o2 + k * (32 * 128));
#pragma unroll
      for (int k = 0; k < 4; ++k) bad |= ((d[k][0] ^ tlo) & 0xffu) | (d[k][1] ^ thi) | ((d[k][2] ^ tlo) & 0xffu) | (d[k][3] ^ thi);
      if (__all(bad == 0)) break;
      if (timeout || ++spins > SPIN_CAP) { timeout = true; break; }
      __builtin_amdgcn_s_sleep(1);
    }
    STAMP(8);
    float y0 = (dec24(d[0][0]) + dec24(d[1][0])) + (dec24(d[2][0]) + dec24(d[3][0]));
    float y1 = (dec24(d[0][2]) + dec24(d[1][2])) + (dec24(d[2][2]) + dec24(d[3][2]));
    float z = swap32_add(y0, y1);
    z = swap16_add(z, z);
    z += dpp<ROR8>(z);
    if ((lane & 31) < 8) s_fin[wave * 16 + 2 * cc + (lane >> 5)] = z;
    wg_barrier();
    if (tid < 8) {
      const float r0 = (s_fin[2 * tid] + s_fin[16 + 2 * tid]) + (s_fin[32 + 2 * tid] + s_fin[48 + 2 * tid]);
      const float r1 = (s_fin[2 * tid + 1] + s_fin[16 + 2 * tid + 1]) + (s_fin[32 + 2 * tid + 1] + s_fin[48 + 2 * tid + 1]);
      u32 o = pack_bf16(r0, r1);
      if (timeout) o = 0x7fc07fc0u;
      *(GAS u32*)((char*)p.out + (fb * 512 + fc * 16 + 2 * tid) * 2) = o;
    }
    if (w == 0 && wave == 0) {
      u32 sp = 0;
      while (true) {
        const u32x4 e0 = ld_sc1(rws, (u32)WS_H2 + ((32 * 128 + lane) * 16) * 8), e1 = ld_sc1(rws, (u32)WS_H2 + ((32 * 128 + 64 + lane) * 16) * 8);
        const u32 bad = ((e0[0] ^ tlo) & 0xffu) | (e0[1] ^ thi) | ((e1[0] ^ tlo) & 0xffu) | (e1[1] ^ thi);
        if (__all(bad == 0)) break;
        if (timeout || ++sp > SPIN_CAP) break;
        __builtin_amdgcn_s_sleep(1);
      }
      if (lane == 0) __hip_atomic_store((gu64*)(p.ws + WS_GEN), tag, __ATOMIC_RELAXED, __HIP_MEMORY_SCOPE_AGENT);
    }
  }
  STAMP(9);
}

extern "C" void kernel_launch(void* const* d_in, const int* in_sizes, int n_in, void* d_out, int out_size, void* d_ws, size_t ws_size,
                              hipStream_t stream) {
  (void)in_sizes; (void)n_in; (void)out_size; (void)ws_size;
  Params p;
  for (int b = 0; b < 2; ++b) {
    p.x[b] = (const char*)d_in[5 * b + 0]; p.k[b] = (const char*)d_in[5 * b + 1]; p.v[b] = (const char*)d_in[5 * b + 2];
    p.wq[b] = (const char*)d_in[5 * b + 3]; p.wo[b] = (const char*)d_in[5 * b + 4];
  }
  p.out = (unsigned short*)d_out; p.ws = (char*)d_ws;
  (void)hipFuncSetAttribute((const void*)concat_heads_attn_kernel, hipFuncAttributeMaxDynamicSharedMemorySize, LDS_BYTES);
  hipLaunchKernelGGL(concat_heads_attn_kernel, dim3(NWG), dim3(NTHR), LDS_BYTES, stream, p);
}
```

```cpp
#define MEASURE 4
#define MEAN 0
#define MULT 0
#include <hip/hip_runtime.h>
#include <hip/hip_bf16.h>
#include <stdint.h>

#define MIMIC_REF 1
#define STAMPS 0

typedef unsigned u32;
typedef unsigned long long u64;
typedef u32 u32x2 __attribute__((ext_vector_type(2)));
typedef u32 u32x4 __attribute__((ext_vector_type(4)));
typedef float f32x4 __attribute__((ext_vector_type(4)));
typedef __bf16 bf16x8 __attribute__((ext_vector_type(8)));
typedef __bf16 bf16x2 __attribute__((ext_vector_type(2)));
#define GAS __attribute__((address_space(1)))
typedef GAS u64 gu64;

constexpr int D_MODEL = 512, HQ = 32, HKV = 8, SK = 65536, DH = 128, WIN = 4096;
constexpr int NWG = 256, NTHR = 256;
constexpr int TILE_KEYS = 32, TILE_BYTES = TILE_KEYS * DH * 2;
constexpr int NPROD_A = 30, NPROD_B = 2;
constexpr float C2 = 0.08838834764831845f * 1.4426950408889634f;

constexpr size_t WS_GEN = 0;
constexpr size_t WS_Q = 4096;
constexpr size_t WS_H1D = WS_Q + 2 * 4096 * 8;
constexpr size_t WS_H1M = WS_H1D + (size_t)2 * 8 * 16 * 32 * 32 * 8;
constexpr size_t WS_H2 = WS_H1M + (size_t)2 * 8 * 16 * 32 * 2 * 8;
constexpr size_t WS_END = WS_H2 + (size_t)2 * 32 * 128 * 16 * 8;
constexpr size_t WS_STAMP = 8u << 20;

#define LDS_KB 84
constexpr int LDS_BYTES = LDS_KB * 1024;
constexpr u32 SPIN_CAP = 1u << 18;

struct Params {
  const char* x[2]; const char* k[2]; const char* v[2]; const char* wq[2]; const char* wo[2];
  unsigned short* out; char* ws;
};

__device__ __forceinline__ __amdgpu_buffer_rsrc_t mkrsrc(const void* p, int bytes) {
  return __builtin_amdgcn_make_buffer_rsrc((void*)p, 0, bytes, 0x00020000);
}
__device__ __forceinline__ u32x4 ld_nt(__amdgpu_buffer_rsrc_t r, u32 off) { return __builtin_amdgcn_raw_buffer_load_b128(r, off, 0, 2); }
__device__ __forceinline__ u32x4 ld_sc1(__amdgpu_buffer_rsrc_t r, u32 off) { return __builtin_amdgcn_raw_buffer_load_b128(r, off, 0, 16); }
__device__ __forceinline__ void st_sc1(__amdgpu_buffer_rsrc_t r, u32 off, u32x4 v) { __builtin_amdgcn_raw_buffer_store_b128(v, r, off, 0, 16); }
__device__ __forceinline__ float dot2(u32 a, u32 b, float c) {
  return __builtin_amdgcn_fdot2_f32_bf16(__builtin_bit_cast(bf16x2, a), __builtin_bit_cast(bf16x2, b), c, false);
}
__device__ __forceinline__ u32 perm_lo(u32 hi, u32 lo) { return __builtin_amdgcn_perm(hi, lo, 0x05040100u); }
__device__ __forceinline__ u32 perm_hi(u32 hi, u32 lo) { return __builtin_amdgcn_perm(hi, lo, 0x07060302u); }
__device__ __forceinline__ u32 pack_bf16(float lo, float hi) {
  bf16x2 r = {(__bf16)lo, (__bf16)hi};
  return __builtin_bit_cast(u32, r);
}
template <int CTRL> __device__ __forceinline__ float dpp(float x) {
  return __builtin_bit_cast(float, __builtin_amdgcn_mov_dpp(__builtin_bit_cast(int, x), CTRL, 0xf, 0xf, true));
}
constexpr int ROR2 = 0x122, ROR4 = 0x124, ROR8 = 0x128;
__device__ __forceinline__ float f(u32 x) { return __uint_as_float(x); }
__device__ __forceinline__ u32 u(float x) { return __float_as_uint(x); }
__device__ __forceinline__ float swap32_add(float a, float b) { auto s = __builtin_amdgcn_permlane32_swap(u(a), u(b), false, false); return f(s[0]) + f(s[1]); }
__device__ __forceinline__ float swap16_add(float a, float b) { auto s = __builtin_amdgcn_permlane16_swap(u(a), u(b), false, false); return f(s[0]) + f(s[1]); }
__device__ __forceinline__ float xrow_max(float x) {
  auto s = __builtin_amdgcn_permlane16_swap(u(x), u(x), false, false); x = fmaxf(f(s[0]), f(s[1]));
  auto t = __builtin_amdgcn_permlane32_swap(u(x), u(x), false, false); return fmaxf(f(t[0]), f(t[1]));
}
__device__ __forceinline__ float xrow_sum(float x) { x = swap16_add(x, x); return swap32_add(x, x); }
__device__ __forceinline__ float ex2(float x) { return __builtin_amdgcn_exp2f(x); }
__device__ __forceinline__ void wg_barrier() { asm volatile("s_waitcnt lgkmcnt(0)\n\ts_barrier" ::: "memory"); }
__device__ __forceinline__ u32 enc24(float v, u32 tlo) { return ((u(v) + 0x80u) & 0xffffff00u) | tlo; }
__device__ __forceinline__ float dec24(u32 lo) { return f(lo & 0xffffff00u); }
#define STAMP(i) do { if ((i) == 0 || (i) == MEASURE) { if (lane == 0) __hip_atomic_store((gu64*)(p.ws + WS_STAMP) + (size_t)(w * 4 + wave) * 2 + ((i) == 0 ? 0 : 1), (u64)__builtin_amdgcn_s_memrealtime(), __ATOMIC_RELAXED, __HIP_MEMORY_SCOPE_AGENT); \
   if ((i) == 0 && MEASURE == 0) { if (lane == 0) __hip_atomic_store((gu64*)(p.ws + WS_STAMP) + (size_t)(w * 4 + wave) * 2 + 1, (u64)__builtin_amdgcn_s_memrealtime(), __ATOMIC_RELAXED, __HIP_MEMORY_SCOPE_AGENT); } } } while (0)

__global__ void __launch_bounds__(NTHR, 2)
concat_heads_attn_kernel(Params p) {
  extern __shared__ __attribute__((aligned(16))) char smem[];
  float* s_qred = (float*)smem;
  u32* s_q = (u32*)(smem + 512);
  float* s_ml = (float*)(smem + 2048);
  float* s_acc = (float*)(smem + 4096);
  u32* s_o = (u32*)(smem + 16384);
  float* s_fin = (float*)(smem + 16640);

  const int tid = threadIdx.x, lane = tid & 63;
  const int wave = __builtin_amdgcn_readfirstlane(tid >> 6);
  const int w = blockIdx.x, g = w & 7, i = w >> 3;
  STAMP(0);
  const int l15 = lane & 15, lq = lane >> 4;

  const int rb = i >> 4;
  const int ab = i >= NPROD_A ? 1 : 0;
  const int pidx = ab ? i - NPROD_A : i;
  const int t0 = ab ? pidx * 64 : pidx * 68 + min(pidx, 8);
  const int nt = ab ? 64 : 68 + (pidx < 8 ? 1 : 0);
  const size_t kvoff = ((size_t)g * SK + (ab ? SK - WIN : 0) + (size_t)t0 * TILE_KEYS) * (DH * 2);
  const char* kp = (ab ? p.k[1] : p.k[0]) + kvoff;
  const char* vp = (ab ? p.v[1] : p.v[0]) + kvoff;

  const __amdgpu_buffer_rsrc_t rws = mkrsrc(p.ws, (int)WS_END);
  const u64 gen = __hip_atomic_load((gu64*)(p.ws + WS_GEN), __ATOMIC_RELAXED, __HIP_MEMORY_SCOPE_AGENT);
  u32x4 xw, wq[8];
  {
    const int rho = lane >> 2, chi = lane & 3;
    const int row0 = 128 * wave + 8 * rho;
    xw = *(const GAS u32x4*)((rb ? p.x[1] : p.x[0]) + row0 * 2);
    const __amdgpu_buffer_rsrc_t rq = mkrsrc((rb ? p.wq[1] : p.wq[0]) + (size_t)(g * 512 + (i & 15) * 32) * 2, D_MODEL * HQ * DH * 2);
    const u32 off = (u32)row0 * (HQ * DH * 2) + chi * 16;
#pragma unroll
    for (int n = 0; n < 8; ++n) wq[n] = ld_nt(rq, off + n * (HQ * DH * 2));
  }
  u32x4 K[2][8], V[2][8];
  const u32 koff0 = (4 * (l15 & 3) + (l15 >> 2)) * 256 + lq * 16, koff1 = koff0 + 4096;
  const u32 voff0 = lane * 16, voff1 = voff0 + 4096;
  auto issue = [&](int r, int k) {
    const int t = wave + 4 * k;
    const int nb = t < nt ? TILE_BYTES : 0;
    const __amdgpu_buffer_rsrc_t rk = mkrsrc(kp + (size_t)t * TILE_BYTES, nb);
    const __amdgpu_buffer_rsrc_t rv = mkrsrc(vp + (size_t)t * TILE_BYTES, nb);
#pragma unroll
    for (int j = 0; j < 4; ++j) K[r][j] = ld_nt(rk, koff0 + j * 64);
#pragma unroll
    for (int j = 0; j < 4; ++j) K[r][4 + j] = ld_nt(rk, koff1 + j * 64);
#pragma unroll
    for (int j = 0; j < 4; ++j) V[r][j] = ld_nt(rv, voff0 + j * 1024);
#pragma unroll
    for (int j = 0; j < 4; ++j) V[r][4 + j] = ld_nt(rv, voff1 + j * 1024);
    __builtin_amdgcn_sched_barrier(0);
  };
  __builtin_amdgcn_sched_barrier(0);
  issue(0, 0);

  {
    float a[8];
#pragma unroll
    for (int c = 0; c < 8; ++c) a[c] = 0.f;
#pragma unroll
    for (int n = 0; n < 8; n += 2)
#pragma unroll
      for (int c = 0; c < 4; ++c) {
        a[2 * c] = dot2(perm_lo(wq[n + 1][c], wq[n][c]), xw[n >> 1], a[2 * c]);
        a[2 * c + 1] = dot2(perm_hi(wq[n + 1][c], wq[n][c]), xw[n >> 1], a[2 * c + 1]);
      }
    STAMP(1);
    float b[4], c2[2];
#pragma unroll
    for (int c = 0; c < 4; ++c) b[c] = swap32_add(a[c], a[c + 4]);
#pragma unroll
    for (int c = 0; c < 2; ++c) c2[c] = swap16_add(b[c], b[c + 2]);
#pragma unroll
    for (int c = 0; c < 2; ++c) { c2[c] += dpp<ROR8>(c2[c]); c2[c] += dpp<ROR4>(c2[c]); }
    if (l15 < 4) *(float2*)(s_qred + wave * 32 + 8 * l15 + 2 * lq) = make_float2(c2[0], c2[1]);
  }
  wg_barrier();
  const u64 tag = gen + 1;
  const u32 thi = (u32)(tag >> 8), tlo = (u32)tag & 0xffu;
  const u32 qhi = (u32)(tag >> 16), qlo = (u32)tag & 0xffffu;
  if (tid < 32) {
    const float q = (s_qred[tid] + s_qred[32 + tid]) + (s_qred[64 + tid] + s_qred[96 + tid]);
    const u32 qb = pack_bf16(q, 0.f) << 16;
    __hip_atomic_store((gu64*)(p.ws + WS_Q) + rb * 4096 + g * 512 + (i & 15) * 32 + tid, ((u64)qhi << 32) | (qb | qlo), __ATOMIC_RELAXED, __HIP_MEMORY_SCOPE_AGENT);
  }
  STAMP(2);
  const u32 qoff = (u32)WS_Q + (ab * 4096 + g * 512 + 2 * tid) * 8;
  bool timeout = false;
  asm volatile("s_waitcnt vmcnt(8)" ::: "memory");
  u32x4 qg = ld_sc1(rws, qoff);
  __builtin_amdgcn_sched_barrier(0);
  issue(1, 1);
  if (!__all(((qg[0] & 0xffffu) == qlo) & (qg[1] == qhi) & ((qg[2] & 0xffffu) == qlo) & (qg[3] == qhi))) {
    u32 spins = 0;
    while (true) {
      qg = ld_sc1(rws, qoff);
      if (__all(((qg[0] & 0xffffu) == qlo) & (qg[1] == qhi) & ((qg[2] & 0xffffu) == qlo) & (qg[3] == qhi))) break;
      if (++spins > SPIN_CAP) { timeout = true; break; }
      __builtin_amdgcn_s_sleep(1);
    }
  }
  s_q[tid] = perm_hi(qg[2], qg[0]);
  wg_barrier();
  u32x4 Q[4];
#pragma unroll
  for (int uu = 0; uu < 4; ++uu) Q[uu] = *(const u32x4*)(s_q + (l15 & 3) * 64 + uu * 16 + lq * 4);
  STAMP(3);

  float m = -INFINITY, l = 0.f;
  f32x4 acc[8];
#pragma unroll
  for (int t = 0; t < 8; ++t) acc[t] = f32x4{0.f, 0.f, 0.f, 0.f};
  auto compute = [&](int r, int k) {
    if (wave + 4 * k < nt) {
      f32x4 s0 = {0.f, 0.f, 0.f, 0.f}, s1 = {0.f, 0.f, 0.f, 0.f};
#pragma unroll
      for (int uu = 0; uu < 4; ++uu) {
        s0 = __builtin_amdgcn_mfma_f32_16x16x32_bf16(__builtin_bit_cast(bf16x8, K[r][uu]), __builtin_bit_cast(bf16x8, Q[uu]), s0, 0, 0, 0);
        s1 = __builtin_amdgcn_mfma_f32_16x16x32_bf16(__builtin_bit_cast(bf16x8, K[r][4 + uu]), __builtin_bit_cast(bf16x8, Q[uu]), s1, 0, 0, 0);
      }
      float s[8] = {s0[0], s0[1], s0[2], s0[3], s1[0], s1[1], s1[2], s1[3]};
#pragma unroll
      for (int j = 0; j < 8; j += 2) {
        u32 a = pack_bf16(s[j], s[j + 1]);
        float x0 = f(a << 16) * 0.08838834764831845f, x1 = f(a & 0xffff0000u) * 0.08838834764831845f;
        a = pack_bf16(x0, x1);
        s[j] = f(a << 16); s[j + 1] = f(a & 0xffff0000u);
      }
      constexpr float CC = 1.4426950408889634f;
      float mx = fmaxf(fmaxf(fmaxf(s[0], s[1]), fmaxf(s[2], s[3])), fmaxf(fmaxf(s[4], s[5]), fmaxf(s[6], s[7])));
      mx = xrow_max(mx);
      const float mn = fmaxf(m, mx);
      if (__any(mn > m)) {
        const float alpha = ex2((m - mn) * CC);
        l *= alpha;
#pragma unroll
        for (int t = 0; t < 8; ++t) acc[t] *= alpha;
        m = mn;
      }
      const float mc = m * CC;
      float pr[8];
#pragma unroll
      for (int j = 0; j < 8; ++j) pr[j] = ex2(fmaf(s[j], CC, -mc));
      l += ((pr[0] + pr[1]) + (pr[2] + pr[3])) + ((pr[4] + pr[5]) + (pr[6] + pr[7]));
      const u32x4 P = {pack_bf16(pr[0], pr[1]), pack_bf16(pr[2], pr[3]), pack_bf16(pr[4], pr[5]), pack_bf16(pr[6], pr[7])};
#pragma unroll
      for (int t = 0; t < 8; ++t) {
        u32x4 A;
#pragma unroll
        for (int jj = 0; jj < 4; ++jj)
          A[jj] = (t & 1) ? perm_hi(V[r][2 * jj + 1][t >> 1], V[r][2 * jj][t >> 1]) : perm_lo(V[r][2 * jj + 1][t >> 1], V[r][2 * jj][t >> 1]);
        acc[t] = __builtin_amdgcn_mfma_f32_16x16x32_bf16(__builtin_bit_cast(bf16x8, A), __builtin_bit_cast(bf16x8, P), acc[t], 0, 0, 0);
      }
    }
    __builtin_amdgcn_sched_barrier(0);
  };
#pragma nounroll
  for (int k = 0; k < 16; k += 2) {
    compute(0, k); issue(0, k + 2);
    compute(1, k + 1); issue(1, k + 3);
  }
  const int hh = (i & 15) >> 2, sl = i & 3;
  u32x4 wo[8];
  {
    const __amdgpu_buffer_rsrc_t ro = mkrsrc((rb ? p.wo[1] : p.wo[0]) + (size_t)((g * 4 + hh) * DH + 32 * sl) * (D_MODEL * 2), 32 * D_MODEL * 2);
#pragma unroll
    for (int n = 0; n < 8; ++n) {
      const int jj = 8 * lq + n;
      wo[n] = ld_nt(ro, (u32)(8 * (jj & 3) + (jj >> 2)) * (D_MODEL * 2) + (128 * wave + 8 * l15) * 2);
    }
    __builtin_amdgcn_sched_barrier(0);
  }
  compute(0, 16);
  compute(1, 17);
  STAMP(4);
  constexpr float CM = 1.4426950408889634f;

  l = xrow_sum(l);
  {
    const int h = l15 & 3, kq = l15 >> 2;
    float* dst = s_acc + (wave * 4 + h) * 128 + 32 * lq + 8 * kq;
    f32x4 a0 = kq == 0 ? acc[0] : kq == 1 ? acc[2] : kq == 2 ? acc[4] : acc[6];
    f32x4 a1 = kq == 0 ? acc[1] : kq == 1 ? acc[3] : kq == 2 ? acc[5] : acc[7];
    *(f32x4*)dst = a0; *(f32x4*)(dst + 4) = a1;
    if (lane < 4) *(float2*)(s_ml + (wave * 4 + lane) * 2) = make_float2(m * CM, l);
  }
  wg_barrier();
  {
    const int h = wave;
    float mw[4], lw[4];
#pragma unroll
    for (int ww = 0; ww < 4; ++ww) { const float2 t = *(const float2*)(s_ml + (ww * 4 + h) * 2); mw[ww] = t.x; lw[ww] = t.y; }
    const float M = dec24(enc24(fmaxf(fmaxf(mw[0], mw[1]), fmaxf(mw[2], mw[3])), 0));
    float o0 = 0.f, o1 = 0.f, L = 0.f;
#pragma unroll
    for (int ww = 0; ww < 4; ++ww) {
      const float fw = ex2(mw[ww] - M);
      const float2 a = *(const float2*)(s_acc + (ww * 4 + h) * 128 + 2 * lane);
      o0 = fmaf(fw, a.x, o0); o1 = fmaf(fw, a.y, o1); L = fmaf(fw, lw[ww], L);
    }
    const int role = (ab * 8 + g) * 16 + h * 4 + (lane >> 4);
    st_sc1(rws, (u32)WS_H1D + ((role * 32 + pidx) * 32 + 2 * l15) * 8, u32x4{enc24(o0, tlo), thi, enc24(o1, tlo), thi});
    if (l15 == 0) st_sc1(rws, (u32)WS_H1M + (role * 32 + pidx) * 16, u32x4{enc24(M, tlo), thi, enc24(L, tlo), thi});
  }
  STAMP(5);

  {
    const int np = rb ? NPROD_B : NPROD_A;
    const int role = (rb * 8 + g) * 16 + (i & 15);
    const int pp = lane >> 1, half = lane & 1;
    const bool live = pp < np;
    const u32 od = (u32)WS_H1D + ((role * 32 + pp) * 32 + 8 * wave + 4 * half) * 8;
    const u32 om = (u32)WS_H1M + (role * 32 + pp) * 16;
    u32x4 d0, d1, ml;
    u32 spins = 0;
    while (true) {
      d0 = ld_sc1(rws, od); d1 = ld_sc1(rws, od + 16); ml = ld_sc1(rws, om);
      const u32 bad = ((d0[0] ^ tlo) & 0xffu) | (d0[1] ^ thi) | ((d0[2] ^ tlo) & 0xffu) | (d0[3] ^ thi) |
                      ((d1[0] ^ tlo) & 0xffu) | (d1[1] ^ thi) | ((d1[2] ^ tlo) & 0xffu) | (d1[3] ^ thi) |
                      ((ml[0] ^ tlo) & 0xffu) | (ml[1] ^ thi) | ((ml[2] ^ tlo) & 0xffu) | (ml[3] ^ thi);
      if (__all(bad == 0 || !live)) break;
      if (timeout || ++spins > SPIN_CAP) { timeout = true; break; }
      __builtin_amdgcn_s_sleep(1);
    }
    STAMP(6);
    const float mp = live ? dec24(ml[0]) : -INFINITY;
    float M = mp;
    M = fmaxf(M, dpp<ROR2>(M)); M = fmaxf(M, dpp<ROR4>(M)); M = fmaxf(M, dpp<ROR8>(M)); M = xrow_max(M);
    const float fw = live ? ex2(mp - M) : 0.f;
    float L = live ? fw * dec24(ml[2]) : 0.f;
    float v0 = live ? fw * dec24(d0[0]) : 0.f, v1 = live ? fw * dec24(d0[2]) : 0.f, v2 = live ? fw * dec24(d1[0]) : 0.f, v3 = live ? fw * dec24(d1[2]) : 0.f;
    L += dpp<ROR2>(L); L += dpp<ROR4>(L); L += dpp<ROR8>(L); L = xrow_sum(L);
    float y0 = swap32_add(v0, v2), y1 = swap32_add(v1, v3);
    float z = swap16_add(y0, y1);
    z += dpp<ROR2>(z); z += dpp<ROR4>(z); z += dpp<ROR8>(z);
    z = z / L;
    if (l15 < 2) ((unsigned short*)s_o)[8 * wave + 4 * l15 + lq] = (unsigned short)(pack_bf16(z, 0.f) & 0xffffu);
  }
  wg_barrier();
  {
    const u32x4 op = *(const u32x4*)(s_o + 4 * lq);
    float a[8];
#pragma unroll
    for (int c = 0; c < 8; ++c) a[c] = 0.f;
#pragma unroll
    for (int n = 0; n < 8; n += 2)
#pragma unroll
      for (int c = 0; c < 4; ++c) {
        a[2 * c] = dot2(perm_lo(wo[n + 1][c], wo[n][c]), op[n >> 1], a[2 * c]);
        a[2 * c + 1] = dot2(perm_hi(wo[n + 1][c], wo[n][c]), op[n >> 1], a[2 * c + 1]);
      }
    float b[4];
#pragma unroll
    for (int c = 0; c < 4; ++c) b[c] = swap32_add(a[c], a[c + 4]);
    const float y0 = swap16_add(b[0], b[2]), y1 = swap16_add(b[1], b[3]);
    const int col = 128 * wave + 8 * l15 + 2 * lq;
    const int prod = g * 16 + (i & 15);
    asm volatile("s_waitcnt vmcnt(0)" ::: "memory");
    wg_barrier();
    st_sc1(rws, (u32)WS_H2 + (((rb * 32 + (col >> 4)) * 128 + prod) * 16 + (col & 15)) * 8, u32x4{enc24(y0, tlo), thi, enc24(y1, tlo), thi});
  }
  STAMP(7);

  if (w < 64) {
    const int fb = w >> 5, fc = w & 31;
    const int cc = tid & 7, pp = tid >> 3;
    const u32 o2 = (u32)WS_H2 + (((fb * 32 + fc) * 128 + pp) * 16 + 2 * cc) * 8;
    u32x4 d[4];
    u32 spins = 0;
    while (true) {
      u32 bad = 0;
#pragma unroll
      for (int k = 0; k < 4; ++k) d[k] = ld_sc1(rws, o2 + k * (32 * 128));
#pragma unroll
      for (int k = 0; k < 4; ++k) bad |= ((d[k][0] ^ tlo) & 0xffu) | (d[k][1] ^ thi) | ((d[k][2] ^ tlo) & 0xffu) | (d[k][3] ^ thi);
      if (__all(bad == 0)) break;
      if (timeout || ++spins > SPIN_CAP) { timeout = true; break; }
      __builtin_amdgcn_s_sleep(1);
    }
    STAMP(8);
    float y0 = (dec24(d[0][0]) + dec24(d[1][0])) + (dec24(d[2][0]) + dec24(d[3][0]));
    float y1 = (dec24(d[0][2]) + dec24(d[1][2])) + (dec24(d[2][2]) + dec24(d[3][2]));
    float z = swap32_add(y0, y1);
    z = swap16_add(z, z);
    z += dpp<ROR8>(z);
    if ((lane & 31) < 8) s_fin[wave * 16 + 2 * cc + (lane >> 5)] = z;
    wg_barrier();
    if (tid < 8) {
      const float r0 = (s_fin[2 * tid] + s_fin[16 + 2 * tid]) + (s_fin[32 + 2 * tid] + s_fin[48 + 2 * tid]);
      const float r1 = (s_fin[2 * tid + 1] + s_fin[16 + 2 * tid + 1]) + (s_fin[32 + 2 * tid + 1] + s_fin[48 + 2 * tid + 1]);
      u32 o = pack_bf16(r0, r1);
      if (timeout) o = 0x7fc07fc0u;
      *(GAS u32*)((char*)p.out + (fb * 512 + fc * 16 + 2 * tid) * 2) = o;
    }
    if (w == 0 && wave == 0) {
      u32 sp = 0;
      while (true) {
        const u32x4 e0 = ld_sc1(rws, (u32)WS_H2 + ((32 * 128 + lane) * 16) * 8), e1 = ld_sc1(rws, (u32)WS_H2 + ((32 * 128 + 64 + lane) * 16) * 8);
        const u32 bad = ((e0[0] ^ tlo) & 0xffu) | (e0[1] ^ thi) | ((e1[0] ^ tlo) & 0xffu) | (e1[1] ^ thi);
        if (__all(bad == 0)) break;
        if (timeout || ++sp > SPIN_CAP) break;
        __builtin_amdgcn_s_sleep(1);
      }
      {
        u64 mn = ~0ull, mx = 0, sum = 0;
        for (int k = lane; k < 1024; k += 64) {
          const u64 a = __hip_atomic_load((gu64*)(p.ws + WS_STAMP) + k * 2, __ATOMIC_RELAXED, __HIP_MEMORY_SCOPE_AGENT);
          const u64 b = __hip_atomic_load((gu64*)(p.ws + WS_STAMP) + k * 2 + 1, __ATOMIC_RELAXED, __HIP_MEMORY_SCOPE_AGENT);
          mn = a < mn ? a : mn; mx = b > mx ? b : mx; sum += b & 0xffffffffull;
        }
        for (int o = 32; o; o >>= 1) { u64 a = __shfl_xor(mn, o), b = __shfl_xor(mx, o), c = __shfl_xor(sum, o); mn = a < mn ? a : mn; mx = b > mx ? b : mx; sum += c; }
        const u64 now = __builtin_amdgcn_s_memrealtime();
        u64 iv = MEASURE == 99 ? now - mn : (MEAN ? (u64)(u32)((u32)(sum / 1024) - (u32)mn) : mx - mn);
        if (iv > 20000) iv = 20000;
        const u64 until = now + iv * MULT;
        while (__builtin_amdgcn_s_memrealtime() < until) __builtin_amdgcn_s_sleep(8);
      }
      if (lane == 0) __hip_atomic_store((gu64*)(p.ws + WS_GEN), tag, __ATOMIC_RELAXED, __HIP_MEMORY_SCOPE_AGENT);
    }
  }
  STAMP(9);
}

extern "C" void kernel_launch(void* const* d_in, const int* in_sizes, int n_in, void* d_out, int out_size, void* d_ws, size_t ws_size,
                              hipStream_t stream) {
  (void)in_sizes; (void)n_in; (void)out_size; (void)ws_size;
  Params p;
  for (int b = 0; b < 2; ++b) {
    p.x[b] = (const char*)d_in[5 * b + 0]; p.k[b] = (const char*)d_in[5 * b + 1]; p.v[b] = (const char*)d_in[5 * b + 2];
    p.wq[b] = (const char*)d_in[5 * b + 3]; p.wo[b] = (const char*)d_in[5 * b + 4];
  }
  p.out = (unsigned short*)d_out; p.ws = (char*)d_ws;
  (void)hipFuncSetAttribute((const void*)concat_heads_attn_kernel, hipFuncAttributeMaxDynamicSharedMemorySize, LDS_BYTES);
  hipLaunchKernelGGL(concat_heads_attn_kernel, dim3(NWG), dim3(NTHR), LDS_BYTES, stream, p);
}
```

```cpp
#include <hip/hip_runtime.h>
#include <hip/hip_bf16.h>
#include <stdint.h>

#define MIMIC_REF 1
#define STAMPS 0

typedef unsigned u32;
typedef unsigned long long u64;
typedef u32 u32x2 __attribute__((ext_vector_type(2)));
typedef u32 u32x4 __attribute__((ext_vector_type(4)));
typedef float f32x4 __attribute__((ext_vector_type(4)));
typedef __bf16 bf16x8 __attribute__((ext_vector_type(8)));
typedef __bf16 bf16x2 __attribute__((ext_vector_type(2)));
#define GAS __attribute__((address_space(1)))
typedef GAS u64 gu64;

constexpr int D_MODEL = 512, HQ = 32, HKV = 8, SK = 65536, DH = 128, WIN = 4096;
constexpr int NWG = 256, NTHR = 256;
constexpr int TILE_KEYS = 32, TILE_BYTES = TILE_KEYS * DH * 2;
constexpr int NPROD_A = 30, NPROD_B = 2;
constexpr float C2 = 0.08838834764831845f * 1.4426950408889634f;

constexpr size_t WS_GEN = 0;
constexpr size_t WS_Q = 4096;
constexpr size_t WS_H1D = WS_Q + 2 * 4096 * 8;
constexpr size_t WS_H1M = WS_H1D + (size_t)2 * 8 * 16 * 32 * 32 * 8;
constexpr size_t WS_H2 = WS_H1M + (size_t)2 * 8 * 16 * 32 * 2 * 8;
constexpr size_t WS_END = WS_H2 + (size_t)2 * 32 * 128 * 16 * 8;
constexpr size_t WS_STAMP = 8u << 20;

#define LDS_KB 84
constexpr int LDS_BYTES = LDS_KB * 1024;
constexpr u32 SPIN_CAP = 1u << 18;

struct Params {
  const char* x[2]; const char* k[2]; const char* v[2]; const char* wq[2]; const char* wo[2];
  unsigned short* out; char* ws;
};

__device__ __forceinline__ __amdgpu_buffer_rsrc_t mkrsrc(const void* p, int bytes) {
  return __builtin_amdgcn_make_buffer_rsrc((void*)p, 0, bytes, 0x00020000);
}
__device__ __forceinline__ u32x4 ld_nt(__amdgpu_buffer_rsrc_t r, u32 off) { return __builtin_amdgcn_raw_buffer_load_b128(r, off, 0, 2); }
__device__ __forceinline__ u32x4 ld_sc1(__amdgpu_buffer_rsrc_t r, u32 off) { return __builtin_amdgcn_raw_buffer_load_b128(r, off, 0, 16); }
__device__ __forceinline__ void st_sc1(__amdgpu_buffer_rsrc_t r, u32 off, u32x4 v) { __builtin_amdgcn_raw_buffer_store_b128(v, r, off, 0, 16); }
__device__ __forceinline__ float dot2(u32 a, u32 b, float c) {
  return __builtin_amdgcn_fdot2_f32_bf16(__builtin_bit_cast(bf16x2, a), __builtin_bit_cast(bf16x2, b), c, false);
}
__device__ __forceinline__ u32 perm_lo(u32 hi, u32 lo) { return __builtin_amdgcn_perm(hi, lo, 0x05040100u); }
__device__ __forceinline__ u32 perm_hi(u32 hi, u32 lo) { return __builtin_amdgcn_perm(hi, lo, 0x07060302u); }
__device__ __forceinline__ u32 pack_bf16(float lo, float hi) {
  bf16x2 r = {(__bf16)lo, (__bf16)hi};
  return __builtin_bit_cast(u32, r);
}
template <int CTRL> __device__ __forceinline__ float dpp(float x) {
  return __builtin_bit_cast(float, __builtin_amdgcn_mov_dpp(__builtin_bit_cast(int, x), CTRL, 0xf, 0xf, true));
}
constexpr int ROR2 = 0x122, ROR4 = 0x124, ROR8 = 0x128;
__device__ __forceinline__ float f(u32 x) { return __uint_as_float(x); }
__device__ __forceinline__ u32 u(float x) { return __float_as_uint(x); }
__device__ __forceinline__ float swap32_add(float a, float b) { auto s = __builtin_amdgcn_permlane32_swap(u(a), u(b), false, false); return f(s[0]) + f(s[1]); }
__device__ __forceinline__ float swap16_add(float a, float b) { auto s = __builtin_amdgcn_permlane16_swap(u(a), u(b), false, false); return f(s[0]) + f(s[1]); }
__device__ __forceinline__ float xrow_max(float x) {
  auto s = __builtin_amdgcn_permlane16_swap(u(x), u(x), false, false); x = fmaxf(f(s[0]), f(s[1]));
  auto t = __builtin_amdgcn_permlane32_swap(u(x), u(x), false, false); return fmaxf(f(t[0]), f(t[1]));
}
__device__ __forceinline__ float xrow_sum(float x) { x = swap16_add(x, x); return swap32_add(x, x); }
__device__ __forceinline__ float ex2(float x) { return __builtin_amdgcn_exp2f(x); }
__device__ __forceinline__ void wg_barrier() { asm volatile("s_waitcnt lgkmcnt(0)\n\ts_barrier" ::: "memory"); }
__device__ __forceinline__ u32 enc24(float v, u32 tlo) { return ((u(v) + 0x80u) & 0xffffff00u) | tlo; }
__device__ __forceinline__ float dec24(u32 lo) { return f(lo & 0xffffff00u); }
#define STAMP(i) do { } while (0)

__global__ void __launch_bounds__(NTHR, 2)
concat_heads_attn_kernel(Params p) {
  extern __shared__ __attribute__((aligned(16))) char smem[];
  float* s_qred = (float*)smem;
  u32* s_q = (u32*)(smem + 512);
  float* s_ml = (float*)(smem + 2048);
  float* s_acc = (float*)(smem + 4096);
  u32* s_o = (u32*)(smem + 16384);
  float* s_fin = (float*)(smem + 16640);

  const int tid = threadIdx.x, lane = tid & 63;
  const int wave = __builtin_amdgcn_readfirstlane(tid >> 6);
  const int w = blockIdx.x, g = w & 7, i = w >> 3;
  STAMP(0);
  const int l15 = lane & 15, lq = lane >> 4;

  const int rb = i >> 4;
  const int ab = i >= NPROD_A ? 1 : 0;
  const int pidx = ab ? i - NPROD_A : i;
  const int t0 = ab ? pidx * 64 : pidx * 68 + min(pidx, 8);
  const int nt = ab ? 64 : 68 + (pidx < 8 ? 1 : 0);
  const size_t kvoff = ((size_t)g * SK + (ab ? SK - WIN : 0) + (size_t)t0 * TILE_KEYS) * (DH * 2);
  const char* kp = (ab ? p.k[1] : p.k[0]) + kvoff;
  const char* vp = (ab ? p.v[1] : p.v[0]) + kvoff;

  const __amdgpu_buffer_rsrc_t rws = mkrsrc(p.ws, (int)WS_END);
  const u64 gen = __hip_atomic_load((gu64*)(p.ws + WS_GEN), __ATOMIC_RELAXED, __HIP_MEMORY_SCOPE_AGENT);
  u32x4 xw, wq[8];
  {
    const int rho = lane >> 2, chi = lane & 3;
    const int row0 = 128 * wave + 8 * rho;
    xw = *(const GAS u32x4*)((rb ? p.x[1] : p.x[0]) + row0 * 2);
    const __amdgpu_buffer_rsrc_t rq = mkrsrc((rb ? p.wq[1] : p.wq[0]) + (size_t)(g * 512 + (i & 15) * 32) * 2, D_MODEL * HQ * DH * 2);
    const u32 off = (u32)row0 * (HQ * DH * 2) + chi * 16;
#pragma unroll
    for (int n = 0; n < 8; ++n) wq[n] = ld_nt(rq, off + n * (HQ * DH * 2));
  }
  u32x4 K[2][8], V[2][8];
  const u32 koff0 = (4 * (l15 & 3) + (l15 >> 2)) * 256 + lq * 16, koff1 = koff0 + 4096;
  const u32 voff0 = lane * 16, voff1 = voff0 + 4096;
  auto issue = [&](int r, int k) {
    const int t = wave + 4 * k;
    const int nb = t < nt ? TILE_BYTES : 0;
    const __amdgpu_buffer_rsrc_t rk = mkrsrc(kp + (size_t)t * TILE_BYTES, nb);
    const __amdgpu_buffer_rsrc_t rv = mkrsrc(vp + (size_t)t * TILE_BYTES, nb);
#pragma unroll
    for (int j = 0; j < 4; ++j) K[r][j] = ld_nt(rk, koff0 + j * 64);
#pragma unroll
    for (int j = 0; j < 4; ++j) K[r][4 + j] = ld_nt(rk, koff1 + j * 64);
#pragma unroll
    for (int j = 0; j < 4; ++j) V[r][j] = ld_nt(rv, voff0 + j * 1024);
#pragma unroll
    for (int j = 0; j < 4; ++j) V[r][4 + j] = ld_nt(rv, voff1 + j * 1024);
    __builtin_amdgcn_sched_barrier(0);
  };
  __builtin_amdgcn_sched_barrier(0);
  issue(0, 0);

  {
    float a[8];
#pragma unroll
    for (int c = 0; c < 8; ++c) a[c] = 0.f;
#pragma unroll
    for (int n = 0; n < 8; n += 2)
#pragma unroll
      for (int c = 0; c < 4; ++c) {
        a[2 * c] = dot2(perm_lo(wq[n + 1][c], wq[n][c]), xw[n >> 1], a[2 * c]);
        a[2 * c + 1] = dot2(perm_hi(wq[n + 1][c], wq[n][c]), xw[n >> 1], a[2 * c + 1]);
      }
    STAMP(1);
    float b[4], c2[2];
#pragma unroll
    for (int c = 0; c < 4; ++c) b[c] = swap32_add(a[c], a[c + 4]);
#pragma unroll
    for (int c = 0; c < 2; ++c) c2[c] = swap16_add(b[c], b[c + 2]);
#pragma unroll
    for (int c = 0; c < 2; ++c) { c2[c] += dpp<ROR8>(c2[c]); c2[c] += dpp<ROR4>(c2[c]); }
    if (l15 < 4) *(float2*)(s_qred + wave * 32 + 8 * l15 + 2 * lq) = make_float2(c2[0], c2[1]);
  }
  wg_barrier();
  const u64 tag = gen + 1;
  const u32 thi = (u32)(tag >> 8), tlo = (u32)tag & 0xffu;
  const u32 qhi = (u32)(tag >> 16), qlo = (u32)tag & 0xffffu;
  if (tid < 32) {
    const float q = (s_qred[tid] + s_qred[32 + tid]) + (s_qred[64 + tid] + s_qred[96 + tid]);
    const u32 qb = pack_bf16(q, 0.f) << 16;
    __hip_atomic_store((gu64*)(p.ws + WS_Q) + rb * 4096 + g * 512 + (i & 15) * 32 + tid, ((u64)qhi << 32) | (qb | qlo), __ATOMIC_RELAXED, __HIP_MEMORY_SCOPE_AGENT);
  }
  STAMP(2);
  const u32 qoff = (u32)WS_Q + (ab * 4096 + g * 512 + 2 * tid) * 8;
  bool timeout = false;
  asm volatile("s_waitcnt vmcnt(8)" ::: "memory");
  u32x4 qg = ld_sc1(rws, qoff);
  __builtin_amdgcn_sched_barrier(0);
  issue(1, 1);
  if (!__all(((qg[0] & 0xffffu) == qlo) & (qg[1] == qhi) & ((qg[2] & 0xffffu) == qlo) & (qg[3] == qhi))) {
    u32 spins = 0;
    while (true) {
      qg = ld_sc1(rws, qoff);
      if (__all(((qg[0] & 0xffffu) == qlo) & (qg[1] == qhi) & ((qg[2] & 0xffffu) == qlo) & (qg[3] == qhi))) break;
      if (++spins > SPIN_CAP) { timeout = true; break; }
      __builtin_amdgcn_s_sleep(1);
    }
  }
  s_q[tid] = perm_hi(qg[2], qg[0]);
  wg_barrier();
  u32x4 Q[4];
#pragma unroll
  for (int uu = 0; uu < 4; ++uu) Q[uu] = *(const u32x4*)(s_q + (l15 & 3) * 64 + uu * 16 + lq * 4);
  STAMP(3);

  float m = -INFINITY, l = 0.f;
  f32x4 acc[8];
#pragma unroll
  for (int t = 0; t < 8; ++t) acc[t] = f32x4{0.f, 0.f, 0.f, 0.f};
  auto compute = [&](int r, int k) {
    if (wave + 4 * k < nt) {
      f32x4 s0 = {0.f, 0.f, 0.f, 0.f}, s1 = {0.f, 0.f, 0.f, 0.f};
#pragma unroll
      for (int uu = 0; uu < 4; ++uu) {
        s0 = __builtin_amdgcn_mfma_f32_16x16x32_bf16(__builtin_bit_cast(bf16x8, K[r][uu]), __builtin_bit_cast(bf16x8, Q[uu]), s0, 0, 0, 0);
        s1 = __builtin_amdgcn_mfma_f32_16x16x32_bf16(__builtin_bit_cast(bf16x8, K[r][4 + uu]), __builtin_bit_cast(bf16x8, Q[uu]), s1, 0, 0, 0);
      }
      float s[8] = {s0[0], s0[1], s0[2], s0[3], s1[0], s1[1], s1[2], s1[3]};
#pragma unroll
      for (int j = 0; j < 8; j += 2) {
        u32 a = pack_bf16(s[j], s[j + 1]);
        float x0 = f(a << 16) * 0.08838834764831845f, x1 = f(a & 0xffff0000u) * 0.08838834764831845f;
        a = pack_bf16(x0, x1);
        s[j] = f(a << 16); s[j + 1] = f(a & 0xffff0000u);
      }
      constexpr float CC = 1.4426950408889634f;
      float mx = fmaxf(fmaxf(fmaxf(s[0], s[1]), fmaxf(s[2], s[3])), fmaxf(fmaxf(s[4], s[5]), fmaxf(s[6], s[7])));
      mx = xrow_max(mx);
      const float mn = fmaxf(m, mx);
      if (__any(mn > m)) {
        const float alpha = ex2((m - mn) * CC);
        l *= alpha;
#pragma unroll
        for (int t = 0; t < 8; ++t) acc[t] *= alpha;
        m = mn;
      }
      const float mc = m * CC;
      float pr[8];
#pragma unroll
      for (int j = 0; j < 8; ++j) pr[j] = ex2(fmaf(s[j], CC, -mc));
      l += ((pr[0] + pr[1]) + (pr[2] + pr[3])) + ((pr[4] + pr[5]) + (pr[6] + pr[7]));
      const u32x4 P = {pack_bf16(pr[0], pr[1]), pack_bf16(pr[2], pr[3]), pack_bf16(pr[4], pr[5]), pack_bf16(pr[6], pr[7])};
#pragma unroll
      for (int t = 0; t < 8; ++t) {
        u32x4 A;
#pragma unroll
        for (int jj = 0; jj < 4; ++jj)
          A[jj] = (t & 1) ? perm_hi(V[r][2 * jj + 1][t >> 1], V[r][2 * jj][t >> 1]) : perm_lo(V[r][2 * jj + 1][t >> 1], V[r][2 * jj][t >> 1]);
        acc[t] = __builtin_amdgcn_mfma_f32_16x16x32_bf16(__builtin_bit_cast(bf16x8, A), __builtin_bit_cast(bf16x8, P), acc[t], 0, 0, 0);
      }
    }
    __builtin_amdgcn_sched_barrier(0);
  };
#pragma nounroll
  for (int k = 0; k < 16; k += 2) {
    compute(0, k); issue(0, k + 2);
    compute(1, k + 1); issue(1, k + 3);
  }
  const int hh = (i & 15) >> 2, sl = i & 3;
  u32x4 wo[8];
  {
    const __amdgpu_buffer_rsrc_t ro = mkrsrc((rb ? p.wo[1] : p.wo[0]) + (size_t)((g * 4 + hh) * DH + 32 * sl) * (D_MODEL * 2), 32 * D_MODEL * 2);
#pragma unroll
    for (int n = 0; n < 8; ++n) {
      const int jj = 8 * lq + n;
      wo[n] = ld_nt(ro, (u32)(8 * (jj & 3) + (jj >> 2)) * (D_MODEL * 2) + (128 * wave + 8 * l15) * 2);
    }
    __builtin_amdgcn_sched_barrier(0);
  }
  compute(0, 16);
  compute(1, 17);
  STAMP(4);
  constexpr float CM = 1.4426950408889634f;

  l = xrow_sum(l);
  {
    const int h = l15 & 3, kq = l15 >> 2;
    float* dst = s_acc + (wave * 4 + h) * 128 + 32 * lq + 8 * kq;
    f32x4 a0 = kq == 0 ? acc[0] : kq == 1 ? acc[2] : kq == 2 ? acc[4] : acc[6];
    f32x4 a1 = kq == 0 ? acc[1] : kq == 1 ? acc[3] : kq == 2 ? acc[5] : acc[7];
    *(f32x4*)dst = a0; *(f32x4*)(dst + 4) = a1;
    if (lane < 4) *(float2*)(s_ml + (wave * 4 + lane) * 2) = make_float2(m * CM, l);
  }
  wg_barrier();
  {
    const int h = wave;
    float mw[4], lw[4];
#pragma unroll
    for (int ww = 0; ww < 4; ++ww) { const float2 t = *(const float2*)(s_ml + (ww * 4 + h) * 2); mw[ww] = t.x; lw[ww] = t.y; }
    const float M = dec24(enc24(fmaxf(fmaxf(mw[0], mw[1]), fmaxf(mw[2], mw[3])), 0));
    float o0 = 0.f, o1 = 0.f, L = 0.f;
#pragma unroll
    for (int ww = 0; ww < 4; ++ww) {
      const float fw = ex2(mw[ww] - M);
      const float2 a = *(const float2*)(s_acc + (ww * 4 + h) * 128 + 2 * lane);
      o0 = fmaf(fw, a.x, o0); o1 = fmaf(fw, a.y, o1); L = fmaf(fw, lw[ww], L);
    }
    const int role = (ab * 8 + g) * 16 + h * 4 + (lane >> 4);
    st_sc1(rws, (u32)WS_H1D + ((role * 32 + pidx) * 32 + 2 * l15) * 8, u32x4{enc24(o0, tlo), thi, enc24(o1, tlo), thi});
    if (l15 == 0) st_sc1(rws, (u32)WS_H1M + (role * 32 + pidx) * 16, u32x4{enc24(M, tlo), thi, enc24(L, tlo), thi});
  }
  STAMP(5);

  {
    const int np = rb ? NPROD_B : NPROD_A;
    const int role = (rb * 8 + g) * 16 + (i & 15);
    const int pp = lane >> 1, half = lane & 1;
    const bool live = pp < np;
    const u32 od = (u32)WS_H1D + ((role * 32 + pp) * 32 + 8 * wave + 4 * half) * 8;
    const u32 om = (u32)WS_H1M + (role * 32 + pp) * 16;
    u32x4 d0, d1, ml;
    u32 spins = 0;
    while (true) {
      d0 = ld_sc1(rws, od); d1 = ld_sc1(rws, od + 16); ml = ld_sc1(rws, om);
      const u32 bad = ((d0[0] ^ tlo) & 0xffu) | (d0[1] ^ thi) | ((d0[2] ^ tlo) & 0xffu) | (d0[3] ^ thi) |
                      ((d1[0] ^ tlo) & 0xffu) | (d1[1] ^ thi) | ((d1[2] ^ tlo) & 0xffu) | (d1[3] ^ thi) |
                      ((ml[0] ^ tlo) & 0xffu) | (ml[1] ^ thi) | ((ml[2] ^ tlo) & 0xffu) | (ml[3] ^ thi);
      if (__all(bad == 0 || !live)) break;
      if (timeout || ++spins > SPIN_CAP) { timeout = true; break; }
      __builtin_amdgcn_s_sleep(1);
    }
    STAMP(6);
    const float mp = live ? dec24(ml[0]) : -INFINITY;
    float M = mp;
    M = fmaxf(M, dpp<ROR2>(M)); M = fmaxf(M, dpp<ROR4>(M)); M = fmaxf(M, dpp<ROR8>(M)); M = xrow_max(M);
    const float fw = live ? ex2(mp - M) : 0.f;
    float L = live ? fw * dec24(ml[2]) : 0.f;
    float v0 = live ? fw * dec24(d0[0]) : 0.f, v1 = live ? fw * dec24(d0[2]) : 0.f, v2 = live ? fw * dec24(d1[0]) : 0.f, v3 = live ? fw * dec24(d1[2]) : 0.f;
    L += dpp<ROR2>(L); L += dpp<ROR4>(L); L += dpp<ROR8>(L); L = xrow_sum(L);
    float y0 = swap32_add(v0, v2), y1 = swap32_add(v1, v3);
    float z = swap16_add(y0, y1);
    z += dpp<ROR2>(z); z += dpp<ROR4>(z); z += dpp<ROR8>(z);
    z = z / L;
    if (l15 < 2) ((unsigned short*)s_o)[8 * wave + 4 * l15 + lq] = (unsigned short)(pack_bf16(z, 0.f) & 0xffffu);
  }
  wg_barrier();
  {
    const u32x4 op = *(const u32x4*)(s_o + 4 * lq);
    float a[8];
#pragma unroll
    for (int c = 0; c < 8; ++c) a[c] = 0.f;
#pragma unroll
    for (int n = 0; n < 8; n += 2)
#pragma unroll
      for (int c = 0; c < 4; ++c) {
        a[2 * c] = dot2(perm_lo(wo[n + 1][c], wo[n][c]), op[n >> 1], a[2 * c]);
        a[2 * c + 1] = dot2(perm_hi(wo[n + 1][c], wo[n][c]), op[n >> 1], a[2 * c + 1]);
      }
    float b[4];
#pragma unroll
    for (int c = 0; c < 4; ++c) b[c] = swap32_add(a[c], a[c + 4]);
    const float y0 = swap16_add(b[0], b[2]), y1 = swap16_add(b[1], b[3]);
    const int col = 128 * wave + 8 * l15 + 2 * lq;
    const int prod = g * 16 + (i & 15);
    st_sc1(rws, (u32)WS_H2 + (((rb * 32 + (col >> 4)) * 128 + prod) * 16 + (col & 15)) * 8, u32x4{enc24(y0, tlo), thi, enc24(y1, tlo), thi});
  }
  STAMP(7);

  if (w < 64) {
    const int fb = w >> 5, fc = w & 31;
    const int cc = tid & 7, pp = tid >> 3;
    const u32 o2 = (u32)WS_H2 + (((fb * 32 + fc) * 128 + pp) * 16 + 2 * cc) * 8;
    u32x4 d[4];
    u32 spins = 0;
    while (true) {
      u32 bad = 0;
#pragma unroll
      for (int k = 0; k < 4; ++k) d[k] = ld_sc1(rws, o2 + k * (32 * 128));
#pragma unroll
      for (int k = 0; k < 4; ++k) bad |= ((d[k][0] ^ tlo) & 0xffu) | (d[k][1] ^ thi) | ((d[k][2] ^ tlo) & 0xffu) | (d[k][3] ^ thi);
      if (__all(bad == 0)) break;
      if (timeout || ++spins > SPIN_CAP) { timeout = true; break; }
      __builtin_amdgcn_s_sleep(1);
    }
    STAMP(8);
    float y0 = (dec24(d[0][0]) + dec24(d[1][0])) + (dec24(d[2][0]) + dec24(d[3][0]));
    float y1 = (dec24(d[0][2]) + dec24(d[1][2])) + (dec24(d[2][2]) + dec24(d[3][2]));
    float z = swap32_add(y0, y1);
    z = swap16_add(z, z);
    z += dpp<ROR8>(z);
    if ((lane & 31) < 8) s_fin[wave * 16 + 2 * cc + (lane >> 5)] = z;
    wg_barrier();
    if (tid < 8) {
      const float r0 = (s_fin[2 * tid] + s_fin[16 + 2 * tid]) + (s_fin[32 + 2 * tid] + s_fin[48 + 2 * tid]);
      const float r1 = (s_fin[2 * tid + 1] + s_fin[16 + 2 * tid + 1]) + (s_fin[32 + 2 * tid + 1] + s_fin[48 + 2 * tid + 1]);
      u32 o = pack_bf16(r0, r1);
      if (timeout) o = 0x7fc07fc0u;
      *(GAS u32*)((char*)p.out + (fb * 512 + fc * 16 + 2 * tid) * 2) = o;
    }
    if (w == 0 && wave == 0) {
      if (lane == 0) __hip_atomic_store((gu64*)(p.ws + WS_GEN), tag, __ATOMIC_RELAXED, __HIP_MEMORY_SCOPE_AGENT);
    }
  }
  STAMP(9);
}

extern "C" void kernel_launch(void* const* d_in, const int* in_sizes, int n_in, void* d_out, int out_size, void* d_ws, size_t ws_size,
                              hipStream_t stream) {
  (void)in_sizes; (void)n_in; (void)out_size; (void)ws_size;
  Params p;
  for (int b = 0; b < 2; ++b) {
    p.x[b] = (const char*)d_in[5 * b + 0]; p.k[b] = (const char*)d_in[5 * b + 1]; p.v[b] = (const char*)d_in[5 * b + 2];
    p.wq[b] = (const char*)d_in[5 * b + 3]; p.wo[b] = (const char*)d_in[5 * b + 4];
  }
  p.out = (unsigned short*)d_out; p.ws = (char*)d_ws;
  (void)hipFuncSetAttribute((const void*)concat_heads_attn_kernel, hipFuncAttributeMaxDynamicSharedMemorySize, LDS_BYTES);
  hipLaunchKernelGGL(concat_heads_attn_kernel, dim3(NWG), dim3(NTHR), LDS_BYTES, stream, p);
}
```

```cpp
#include <hip/hip_runtime.h>
#include <hip/hip_bf16.h>
#include <stdint.h>

#define STAMPS 0
#define LDS_KB 84
#define WQ_WAIT 4
#define SLEEPL 8
#define SLEEPN 24
#define STR_(x) #x
#define XSTR(x) STR_(x)

typedef unsigned u32;
typedef unsigned long long u64;
typedef u32 u32x4 __attribute__((ext_vector_type(4)));
typedef float f32x2 __attribute__((ext_vector_type(2)));
typedef float f32x4 __attribute__((ext_vector_type(4)));
typedef __bf16 bf16x8 __attribute__((ext_vector_type(8)));
typedef __bf16 bf16x2 __attribute__((ext_vector_type(2)));
#define GAS __attribute__((address_space(1)))
typedef GAS u64 gu64;

constexpr int D_MODEL = 512, HQ = 32, SK = 65536, DH = 128, WIN = 4096;
constexpr int NWG = 256, NTHR = 256;
constexpr int TILE_KEYS = 32, TILE_BYTES = TILE_KEYS * DH * 2;
constexpr int NPROD_A = 30, NPROD_B = 2;
constexpr float LOG2E = 1.4426950408889634f, SCALE = 0.08838834764831845f;
constexpr float THR = 8.0f / LOG2E;

constexpr size_t WS_GEN = 0;
constexpr size_t WS_Q = 4096;
constexpr size_t WS_H1D = WS_Q + (size_t)2 * 8 * 2 * 512 * 8;
constexpr size_t WS_H1M = WS_H1D + (size_t)2 * 8 * 16 * 32 * 32 * 8;
constexpr size_t WS_H2 = WS_H1M + (size_t)2 * 8 * 16 * 32 * 2 * 8;
constexpr size_t WS_END = WS_H2 + (size_t)2 * 32 * 128 * 16 * 8;
constexpr size_t WS_STAMP = 8u << 20;

constexpr int LDS_BYTES = LDS_KB * 1024;
constexpr u32 SPIN_CAP = 1u << 18;

struct Params {
  const char* x[2]; const char* wq[2]; const char* k[2]; const char* v[2]; char* ws; const char* wo[2];
  unsigned short* out;
};

__device__ __forceinline__ __amdgpu_buffer_rsrc_t mkrsrc(const void* p, int bytes) {
  return __builtin_amdgcn_make_buffer_rsrc((void*)p, 0, bytes, 0x00020000);
}
__device__ __forceinline__ u32x4 ld_nt(__amdgpu_buffer_rsrc_t r, u32 off) { return __builtin_amdgcn_raw_buffer_load_b128(r, off, 0, 2); }
__device__ __forceinline__ u32x4 ld_sc1(__amdgpu_buffer_rsrc_t r, u32 off) { return __builtin_amdgcn_raw_buffer_load_b128(r, off, 0, 16); }
__device__ __forceinline__ void st_sc1(__amdgpu_buffer_rsrc_t r, u32 off, u32x4 v) { __builtin_amdgcn_raw_buffer_store_b128(v, r, off, 0, 16); }
__device__ __forceinline__ float dot2(u32 a, u32 b, float c) {
  return __builtin_amdgcn_fdot2_f32_bf16(__builtin_bit_cast(bf16x2, a), __builtin_bit_cast(bf16x2, b), c, false);
}
__device__ __forceinline__ f32x4 mfma(u32x4 a, u32x4 b, f32x4 c) {
  return __builtin_amdgcn_mfma_f32_16x16x32_bf16(__builtin_bit_cast(bf16x8, a), __builtin_bit_cast(bf16x8, b), c, 0, 0, 0);
}
__device__ __forceinline__ u32 perm_lo(u32 hi, u32 lo) { return __builtin_amdgcn_perm(hi, lo, 0x05040100u); }
__device__ __forceinline__ u32 perm_hi(u32 hi, u32 lo) { return __builtin_amdgcn_perm(hi, lo, 0x07060302u); }
__device__ __forceinline__ u32x4 colfrag(const u32x4 (&r)[8], int t) {
  u32x4 a;
#pragma unroll
  for (int jj = 0; jj < 4; ++jj) a[jj] = (t & 1) ? perm_hi(r[2 * jj + 1][t >> 1], r[2 * jj][t >> 1]) : perm_lo(r[2 * jj + 1][t >> 1], r[2 * jj][t >> 1]);
  return a;
}
__device__ __forceinline__ u32 pack_bf16(float lo, float hi) { bf16x2 r = {(__bf16)lo, (__bf16)hi}; return __builtin_bit_cast(u32, r); }
__device__ __forceinline__ u32 cvtpk(float lo, float hi) { u32 r; asm("v_cvt_pk_bf16_f32 %0, %1, %2" : "=v"(r) : "v"(lo), "v"(hi)); return r; }
template <int CTRL> __device__ __forceinline__ float dpp(float x) {
  return __builtin_bit_cast(float, __builtin_amdgcn_mov_dpp(__builtin_bit_cast(int, x), CTRL, 0xf, 0xf, true));
}
constexpr int ROR2 = 0x122, ROR4 = 0x124, ROR8 = 0x128;
__device__ __forceinline__ float f(u32 x) { return __uint_as_float(x); }
__device__ __forceinline__ u32 u(float x) { return __float_as_uint(x); }
__device__ __forceinline__ float swap32_add(float a, float b) { auto s = __builtin_amdgcn_permlane32_swap(u(a), u(b), false, false); return f(s[0]) + f(s[1]); }
__device__ __forceinline__ float swap16_add(float a, float b) { auto s = __builtin_amdgcn_permlane16_swap(u(a), u(b), false, false); return f(s[0]) + f(s[1]); }
__device__ __forceinline__ float xrow_sum(float x) { x = swap16_add(x, x); return swap32_add(x, x); }
__device__ __forceinline__ float vmax0(float a, float b) { float r; asm("v_max_f32 %0, %1, %2" : "=v"(r) : "v"(a), "v"(b)); return r; }
__device__ __forceinline__ float vmax(float a, float b) { float r; asm("v_max_f32 %0, %1, %2\n\ts_nop 1" : "=v"(r) : "v"(a), "v"(b)); return r; }
__device__ __forceinline__ float xrow_max(float x) {
  { auto s = __builtin_amdgcn_permlane16_swap(u(x), u(x), false, false); x = vmax(f(s[0]), f(s[1])); }
  { auto s = __builtin_amdgcn_permlane32_swap(u(x), u(x), false, false); x = vmax(f(s[0]), f(s[1])); }
  return x;
}
#define DPP_MAX(NAME, CTRL) __device__ __forceinline__ float NAME(float x) { float r; \
  asm("s_nop 1\n\tv_max_f32_dpp %0, %1, %1 " CTRL " row_mask:0xf bank_mask:0xf\n\ts_nop 1" : "=&v"(r) : "v"(x)); return r; }
DPP_MAX(max_ror2, "row_ror:2") DPP_MAX(max_ror4, "row_ror:4") DPP_MAX(max_ror8, "row_ror:8")
__device__ __forceinline__ float ex2(float x) { return __builtin_amdgcn_exp2f(x); }
__device__ __forceinline__ void wg_barrier() { asm volatile("s_waitcnt lgkmcnt(0)\n\ts_barrier" ::: "memory"); }
__device__ __forceinline__ u32 enc24(float v, u32 tlo) { return ((u(v) + 0x80u) & 0xffffff00u) | tlo; }
__device__ __forceinline__ float dec24(u32 lo) { return f(lo & 0xffffff00u); }
__device__ __forceinline__ u32 bad40(const u32x4& g, u32 tlo, u32 thi) { return (((g[0] ^ tlo) | (g[2] ^ tlo)) & 0xffu) | (g[1] ^ thi) | (g[3] ^ thi); }
#define FENCE() __builtin_amdgcn_sched_barrier(0)
#define STAMP(i) do { } while (0)
#define LSTAMP(kk) do { } while (0)

__global__ void __launch_bounds__(NTHR, 2)
concat_heads_attn_kernel(Params p) {
  extern __shared__ __attribute__((aligned(16))) char smem[];
  constexpr int ACC_LD = 132;
  float* s_qred = (float*)smem;
  u32* s_q = (u32*)(smem + 2048);
  float* s_ml = (float*)(smem + 3072);
  float* s_acc = (float*)(smem + 4096);
  u32* s_o = (u32*)(smem + 40960);
  float* s_fin = (float*)(smem + 41216);
  u32* s_flag = (u32*)(smem + 41984);

  const int tid = threadIdx.x, lane = tid & 63;
  const int wave = __builtin_amdgcn_readfirstlane(tid >> 6);
  const int w = blockIdx.x, g = w & 7, i = w >> 3;
  STAMP(0);
  const int l15 = lane & 15, lq = lane >> 4;
  const int rb = i >> 4;

  u32x4 xw, wq[8];
  {
    const int row0 = 256 * ((i & 15) >> 3) + 64 * wave + 8 * (lane >> 3);
    const __amdgpu_buffer_rsrc_t rq = mkrsrc((rb ? p.wq[1] : p.wq[0]) + (size_t)(g * 512 + (i & 7) * 64) * 2, D_MODEL * HQ * DH * 2);
    const u32 off = (u32)row0 * (HQ * DH * 2) + (lane & 7) * 16;
#pragma unroll
    for (int n = 0; n < 8; ++n) wq[n] = ld_nt(rq, off + n * (HQ * DH * 2));
    xw = *(const GAS u32x4*)((rb ? p.x[1] : p.x[0]) + row0 * 2);
    FENCE();
  }
  u64 gen;
  asm volatile("s_load_dwordx2 %0, %1, 0x0" : "=s"(gen) : "s"(p.ws) : "memory");

  const int ab = i >= NPROD_A ? 1 : 0;
  const int pidx = ab ? i - NPROD_A : i;
  const int t0 = ab ? pidx * 64 : pidx * 68 + min(pidx, 8);
  const int nt = ab ? 64 : 68 + (pidx < 8 ? 1 : 0);
  const size_t kvoff = ((size_t)g * SK + (ab ? SK - WIN : 0) + (size_t)t0 * TILE_KEYS) * (DH * 2);
  const char* kp = (ab ? p.k[1] : p.k[0]) + kvoff;
  const char* vp = (ab ? p.v[1] : p.v[0]) + kvoff;
  const __amdgpu_buffer_rsrc_t rws = mkrsrc(p.ws, (int)WS_END);

  u32x4 K[2][8], V[2][8];
  const u32 koff0 = (4 * (l15 & 3) + (l15 >> 2)) * 256 + lq * 16, koff1 = koff0 + 4096;
  const u32 voff0 = lane * 16, voff1 = voff0 + 4096;
  auto issueK = [&](int r, int k, int half) {
    const int t = wave + 4 * k;
    const __amdgpu_buffer_rsrc_t rk = mkrsrc(kp + (size_t)t * TILE_BYTES, t < nt ? TILE_BYTES : 0);
    if (half != 1) {
#pragma unroll
      for (int j = 0; j < 4; ++j) K[r][j] = ld_nt(rk, koff0 + j * 64);
    }
    if (half != 0) {
#pragma unroll
      for (int j = 0; j < 4; ++j) K[r][4 + j] = ld_nt(rk, koff1 + j * 64);
    }
    FENCE();
  };
  auto issueV = [&](int r, int k) {
    const int t = wave + 4 * k;
    const __amdgpu_buffer_rsrc_t rv = mkrsrc(vp + (size_t)t * TILE_BYTES, t < nt ? TILE_BYTES : 0);
#pragma unroll
    for (int j = 0; j < 4; ++j) V[r][j] = ld_nt(rv, voff0 + j * 1024);
#pragma unroll
    for (int j = 0; j < 4; ++j) V[r][4 + j] = ld_nt(rv, voff1 + j * 1024);
    FENCE();
  };
  FENCE();
  asm volatile("s_waitcnt vmcnt(" XSTR(WQ_WAIT) ")" ::: "memory");
  FENCE();
  issueK(0, 0, 0);

  {
    float a[8];
#pragma unroll
    for (int c = 0; c < 8; ++c) a[c] = 0.f;
#pragma unroll
    for (int n = 0; n < 8; n += 2)
#pragma unroll
      for (int c = 0; c < 4; ++c) {
        a[2 * c] = dot2(perm_lo(wq[n + 1][c], wq[n][c]), xw[n >> 1], a[2 * c]);
        a[2 * c + 1] = dot2(perm_hi(wq[n + 1][c], wq[n][c]), xw[n >> 1], a[2 * c + 1]);
      }
    STAMP(1);
    float b[4];
#pragma unroll
    for (int c = 0; c < 4; ++c) b[c] = swap32_add(a[c], a[c + 4]);
    float y0 = swap16_add(b[0], b[2]), y1 = swap16_add(b[1], b[3]);
    y0 += dpp<ROR8>(y0); y1 += dpp<ROR8>(y1);
    if (l15 < 8) *(float2*)(s_qred + wave * 64 + 8 * l15 + 2 * lq) = make_float2(y0, y1);
  }
  wg_barrier();
  asm volatile("s_waitcnt lgkmcnt(0)" : "+s"(gen) :: "memory");
  const u64 tag = gen + 1;
  const u32 thi = (u32)(tag >> 8), tlo = (u32)tag & 0xffu;
  const u32 qtag = (u32)tag;
  if (tid < 32) {
    const float2 a0 = *(const float2*)(s_qred + 2 * lane), a1 = *(const float2*)(s_qred + 64 + 2 * lane);
    const float2 a2 = *(const float2*)(s_qred + 128 + 2 * lane), a3 = *(const float2*)(s_qred + 192 + 2 * lane);
    st_sc1(rws, (u32)WS_Q + (((rb * 8 + g) * 2 + ((i & 15) >> 3)) * 512 + (i & 7) * 64 + 2 * lane) * 8,
           u32x4{u((a0.x + a1.x) + (a2.x + a3.x)), qtag, u((a0.y + a1.y) + (a2.y + a3.y)), qtag});
  }
  STAMP(2);

  const u32 qoff = (u32)WS_Q + ((ab * 8 + g) * 2 * 512 + 2 * tid) * 8, qoff1 = qoff + 512 * 8;
  bool timeout = false, okw = false, have = false;
  u32x4 qa0 = {0u, 0u, 0u, 0u}, qa1 = qa0;
  const u32x4 srd_ws = {(u32)(uintptr_t)p.ws, (u32)((uintptr_t)p.ws >> 32) & 0xffffu, (u32)WS_END, 0x00020000u};
#define Q_POLL() do { \
    asm volatile("s_nop 4\n\tbuffer_load_dwordx4 %0, %2, %4, 0 offen sc1\n\tbuffer_load_dwordx4 %1, %3, %4, 0 offen sc1" \
                 : "+v"(qa0), "+v"(qa1) : "v"(qoff), "v"(qoff1), "s"(srd_ws) : "memory"); \
    FENCE(); } while (0)
#define Q_WAIT(N) asm volatile("s_waitcnt vmcnt(" #N ")" : "+v"(qa0), "+v"(qa1) :: "memory")
  auto q_extract = [&]() {
    const u32 bad = (qa0[1] ^ qtag) | (qa0[3] ^ qtag) | (qa1[1] ^ qtag) | (qa1[3] ^ qtag);
    if (__all(bad == 0)) {
      okw = true;
      s_q[tid] = pack_bf16(f(qa0[0]) + f(qa1[0]), f(qa0[2]) + f(qa1[2]));
    }
  };
  auto q_sync = [&](int it) {
    if (lane == 0) s_flag[(it & 1) * 4 + wave] = okw ? 1u : 0u;
    wg_barrier();
    const u32x4 fl = *(const u32x4*)(s_flag + (it & 1) * 4);
    have = __builtin_amdgcn_readfirstlane(fl[0] & fl[1] & fl[2] & fl[3]) != 0;
  };
  issueK(0, 0, 1); issueV(0, 0); issueK(1, 1, 2);
  asm volatile("s_barrier" ::: "memory");
  STAMP(10);
  Q_POLL();
  asm volatile("s_barrier" ::: "memory");
  issueV(1, 1);
  Q_WAIT(8);
  STAMP(11);
  q_extract();
  q_sync(0);
  for (u32 spins = 0; !have; ++spins) {
    if (spins > SPIN_CAP) { timeout = true; break; }
    __builtin_amdgcn_s_sleep(1);
    if (!okw) { Q_POLL(); Q_WAIT(0); q_extract(); }
    q_sync(spins + 1);
    STAMP(15);
  }
  u32x4 Q[4];
#pragma unroll
  for (int uu = 0; uu < 4; ++uu) Q[uu] = *(const u32x4*)(s_q + (l15 & 3) * 64 + uu * 16 + lq * 4);
  STAMP(3);

  const int kq = l15 >> 2;
  const bool hiT = kq >= 2, hiR = (kq & 1) != 0;
  float m = -INFINITY, mc = -INFINITY, mthr = -INFINITY, l = 0.f;
  f32x4 acc[8];
#pragma unroll
  for (int t = 0; t < 8; ++t) acc[t] = f32x4{0.f, 0.f, 0.f, 0.f};
  auto compute = [&](int r, int k) {
    if (wave + 4 * k < nt) {
      f32x4 s0 = {0.f, 0.f, 0.f, 0.f}, s1 = {0.f, 0.f, 0.f, 0.f};
#pragma unroll
      for (int uu = 0; uu < 4; ++uu) { s0 = mfma(K[r][uu], Q[uu], s0); s1 = mfma(K[r][4 + uu], Q[uu], s1); }
      FENCE();
      u32x4 A0 = colfrag(V[r], 0), A1 = colfrag(V[r], 1);
      asm volatile("" : "+v"(A0), "+v"(A1));
      FENCE();
      const float t0_ = hiT ? s1[0] : s0[0], t1_ = hiT ? s1[1] : s0[1], t2_ = hiT ? s1[2] : s0[2], t3_ = hiT ? s1[3] : s0[3];
      const float x0 = hiR ? t2_ : t0_, x1 = hiR ? t3_ : t1_;
      const u32 a = cvtpk(x0, x1);
      const f32x2 y = f32x2{f(a << 16), f(a & 0xffff0000u)} * SCALE;
      const u32 b = cvtpk(y[0], y[1]);
      const float z0 = f(b << 16), z1 = f(b & 0xffff0000u);
      if (__any(vmax0(z0, z1) > mthr)) {
        const float mn = vmax(m, xrow_max(vmax(z0, z1)));
        const float alpha = ex2((m - mn) * LOG2E);
        l *= alpha;
#pragma unroll
        for (int t = 0; t < 8; ++t) acc[t] *= alpha;
        m = mn; mc = mn * LOG2E; mthr = mn + THR;
      }
      const float p0 = ex2(fmaf(z0, LOG2E, -mc)), p1 = ex2(fmaf(z1, LOG2E, -mc));
      const u32 pp = pack_bf16(p0, p1);
      const u32x4 P = {kq == 0 ? pp : 0u, kq == 1 ? pp : 0u, kq == 2 ? pp : 0u, kq == 3 ? pp : 0u};
      l = dot2(pp, 0x3f803f80u, l);
      FENCE();
#pragma unroll
      for (int t = 0; t < 8; t += 2) {
        acc[t] = mfma(A0, P, acc[t]);
        FENCE();
        if (t + 2 < 8) A0 = colfrag(V[r], t + 2);
        FENCE();
        acc[t + 1] = mfma(A1, P, acc[t + 1]);
        FENCE();
        if (t + 3 < 8) A1 = colfrag(V[r], t + 3);
        FENCE();
      }
    }
    FENCE();
  };
#pragma nounroll
  for (int k = 0; k < 16; k += 2) {
    compute(0, k); LSTAMP(k); issueK(0, k + 2, 2); issueV(0, k + 2);
    compute(1, k + 1); LSTAMP(k + 1); issueK(1, k + 3, 2); issueV(1, k + 3);
  }
  compute(0, 16); LSTAMP(16);
  compute(1, 17); LSTAMP(17);
  STAMP(4);
  const int hh = (i & 15) >> 2, sl = i & 3;
  u32x4 wo[8];
  {
    const __amdgpu_buffer_rsrc_t ro = mkrsrc((rb ? p.wo[1] : p.wo[0]) + (size_t)((g * 4 + hh) * DH + 32 * sl) * (D_MODEL * 2), 32 * D_MODEL * 2);
#pragma unroll
    for (int n = 0; n < 8; ++n) {
      const int jj = 8 * lq + n;
      wo[n] = ld_nt(ro, (u32)(8 * (jj & 3) + (jj >> 2)) * (D_MODEL * 2) + (128 * wave + 8 * l15) * 2);
    }
    FENCE();
  }

  {
    const float M = max_ror8(max_ror4(m));
    const float fs = ex2((m - M) * LOG2E);
    l *= fs;
    l += dpp<ROR8>(l); l += dpp<ROR4>(l);
    l = xrow_sum(l);
    float* dst = s_acc + (wave * 16 + l15) * ACC_LD + 32 * lq;
#pragma unroll
    for (int t = 0; t < 8; ++t) *(f32x4*)(dst + 4 * t) = acc[t] * fs;
    if (lane < 4) *(float2*)(s_ml + (wave * 4 + lane) * 2) = make_float2(M * LOG2E, l);
  }
  wg_barrier();
  {
    const int h = wave;
    float mw[4], lw[4];
    float2 a[4][4];
#pragma unroll
    for (int ww = 0; ww < 4; ++ww) { const float2 t = *(const float2*)(s_ml + (ww * 4 + h) * 2); mw[ww] = t.x; lw[ww] = t.y; }
#pragma unroll
    for (int ww = 0; ww < 4; ++ww)
#pragma unroll
      for (int k = 0; k < 4; ++k) a[ww][k] = *(const float2*)(s_acc + (ww * 16 + 4 * k + h) * ACC_LD + 2 * lane);
    const float M = dec24(enc24(vmax0(vmax0(mw[0], mw[1]), vmax0(mw[2], mw[3])), 0));
    float o0 = 0.f, o1 = 0.f, L = 0.f;
#pragma unroll
    for (int ww = 0; ww < 4; ++ww) {
      const float fw = ex2(mw[ww] - M);
      o0 = fmaf(fw, (a[ww][0].x + a[ww][1].x) + (a[ww][2].x + a[ww][3].x), o0);
      o1 = fmaf(fw, (a[ww][0].y + a[ww][1].y) + (a[ww][2].y + a[ww][3].y), o1);
      L = fmaf(fw, lw[ww], L);
    }
    const int role = (ab * 8 + g) * 16 + h * 4 + lq;
    st_sc1(rws, (u32)WS_H1D + ((role * 32 + pidx) * 32 + 2 * l15) * 8, u32x4{enc24(o0, tlo), thi, enc24(o1, tlo), thi});
    if (l15 == 0) st_sc1(rws, (u32)WS_H1M + (role * 32 + pidx) * 16, u32x4{enc24(M, tlo), thi, enc24(L, tlo), thi});
  }
  STAMP(5);

  u32x4 B[8];
#pragma unroll
  for (int t = 0; t < 8; ++t) B[t] = colfrag(wo, t);
  {
    asm volatile("" : "+v"(B[0]), "+v"(B[1]), "+v"(B[2]), "+v"(B[3]), "+v"(B[4]), "+v"(B[5]), "+v"(B[6]), "+v"(B[7]));
    const int np = rb ? NPROD_B : NPROD_A;
    const int role = (rb * 8 + g) * 16 + (i & 15);
    const int pp = lane >> 1, half = lane & 1;
    const u32 od = (u32)WS_H1D + ((role * 32 + pp) * 32 + 8 * wave + 4 * half) * 8;
    const u32 om = (u32)WS_H1M + (role * 32 + pp) * 16;
    bool miss = pp < np;
    u32x4 d0 = {0u, 0u, 0u, 0u}, d1 = d0, ml = {0xff800000u, 0u, 0u, 0u};
    for (u32 spins = 0;; ++spins) {
      if (miss) {
        d0 = ld_sc1(rws, od); d1 = ld_sc1(rws, od + 16); ml = ld_sc1(rws, om);
        miss = (bad40(d0, tlo, thi) | bad40(d1, tlo, thi) | bad40(ml, tlo, thi)) != 0;
      }
      const u64 mm = __ballot(miss);
      if (mm == 0) break;
      if (timeout || spins > SPIN_CAP) { timeout = true; break; }
      if (__popcll(mm) > SLEEPN) __builtin_amdgcn_s_sleep(SLEEPL); else __builtin_amdgcn_s_sleep(1);
    }
    STAMP(6);
    const float mp = dec24(ml[0]);
    const float M = xrow_max(max_ror8(max_ror4(max_ror2(mp))));
    const float fw = ex2(mp - M);
    float L = fw * dec24(ml[2]);
    const float v0 = fw * dec24(d0[0]), v1 = fw * dec24(d0[2]), v2 = fw * dec24(d1[0]), v3 = fw * dec24(d1[2]);
    L += dpp<ROR2>(L); L += dpp<ROR4>(L); L += dpp<ROR8>(L); L = xrow_sum(L);
    const float y0 = swap32_add(v0, v2), y1 = swap32_add(v1, v3);
    float z = swap16_add(y0, y1);
    z += dpp<ROR2>(z); z += dpp<ROR4>(z); z += dpp<ROR8>(z);
    z *= __builtin_amdgcn_rcpf(L);
    if (l15 < 2) ((unsigned short*)s_o)[8 * wave + 4 * l15 + lq] = (unsigned short)(pack_bf16(z, 0.f) & 0xffffu);
  }
  wg_barrier();
  {
    const u32x4 op = *(const u32x4*)(s_o + 4 * lq);
    f32x4 d[8];
#pragma unroll
    for (int t = 0; t < 8; ++t) d[t] = mfma(op, B[t], f32x4{0.f, 0.f, 0.f, 0.f});
    const float y0 = lq == 0 ? d[0][0] : lq == 1 ? d[2][0] : lq == 2 ? d[4][0] : d[6][0];
    const float y1 = lq == 0 ? d[1][0] : lq == 1 ? d[3][0] : lq == 2 ? d[5][0] : d[7][0];
    const int col = 128 * wave + 8 * l15 + 2 * lq;
    const int prod = g * 16 + (i & 15);
    st_sc1(rws, (u32)WS_H2 + (((rb * 32 + (col >> 4)) * 128 + prod) * 16 + (col & 15)) * 8, u32x4{enc24(y0, tlo), thi, enc24(y1, tlo), thi});
  }
  STAMP(7);

  if (w < 64) {
    const int fb = w >> 5, fc = w & 31;
    const int cc = tid & 7, pp = tid >> 3;
    const u32 o2 = (u32)WS_H2 + (((fb * 32 + fc) * 128 + pp) * 16 + 2 * cc) * 8;
    u32x4 d[4];
    bool miss[4] = {true, true, true, true};
#pragma unroll
    for (int k = 0; k < 4; ++k) d[k] = u32x4{0u, 0u, 0u, 0u};
    for (u32 spins = 0;; ++spins) {
#pragma unroll
      for (int k = 0; k < 4; ++k) if (miss[k]) d[k] = ld_sc1(rws, o2 + k * (32 * 128));
#pragma unroll
      for (int k = 0; k < 4; ++k) miss[k] = bad40(d[k], tlo, thi) != 0;
      const int nm = __popcll(__ballot(miss[0])) + __popcll(__ballot(miss[1])) + __popcll(__ballot(miss[2])) + __popcll(__ballot(miss[3]));
      if (nm == 0) break;
      if (timeout || spins > SPIN_CAP) { timeout = true; break; }
      if (nm > 4 * SLEEPN) __builtin_amdgcn_s_sleep(SLEEPL); else __builtin_amdgcn_s_sleep(1);
    }
    STAMP(8);
    const float y0 = (dec24(d[0][0]) + dec24(d[1][0])) + (dec24(d[2][0]) + dec24(d[3][0]));
    const float y1 = (dec24(d[0][2]) + dec24(d[1][2])) + (dec24(d[2][2]) + dec24(d[3][2]));
    float z = swap32_add(y0, y1);
    z = swap16_add(z, z);
    z += dpp<ROR8>(z);
    if ((lane & 31) < 8) s_fin[wave * 16 + 2 * cc + (lane >> 5)] = z;
    wg_barrier();
    if (tid < 8) {
      const float r0 = (s_fin[2 * tid] + s_fin[16 + 2 * tid]) + (s_fin[32 + 2 * tid] + s_fin[48 + 2 * tid]);
      const float r1 = (s_fin[2 * tid + 1] + s_fin[16 + 2 * tid + 1]) + (s_fin[32 + 2 * tid + 1] + s_fin[48 + 2 * tid + 1]);
      u32 o = pack_bf16(r0, r1);
      if (timeout) o = 0x7fc07fc0u;
      *(GAS u32*)((char*)p.out + (fb * 512 + fc * 16 + 2 * tid) * 2) = o;
    }
    if (w == 0 && wave == 0) {
      for (u32 sp = 0;; ++sp) {
        const u32x4 e0 = ld_sc1(rws, (u32)WS_H2 + ((32 * 128 + lane) * 16) * 8), e1 = ld_sc1(rws, (u32)WS_H2 + ((32 * 128 + 64 + lane) * 16) * 8);
        if (__all((bad40(e0, tlo, thi) | bad40(e1, tlo, thi)) == 0)) break;
        if (timeout || sp > SPIN_CAP) break;
        __builtin_amdgcn_s_sleep(1);
      }
      if (lane == 0) __hip_atomic_store((gu64*)(p.ws + WS_GEN), tag, __ATOMIC_RELAXED, __HIP_MEMORY_SCOPE_AGENT);
    }
  }
  STAMP(9);
}

extern "C" void kernel_launch(void* const* d_in, const int* in_sizes, int n_in, void* d_out, int out_size, void* d_ws, size_t ws_size,
                              hipStream_t stream) {
  (void)in_sizes; (void)n_in; (void)out_size; (void)ws_size;
  Params p;
  for (int b = 0; b < 2; ++b) {
    p.x[b] = (const char*)d_in[5 * b + 0]; p.k[b] = (const char*)d_in[5 * b + 1]; p.v[b] = (const char*)d_in[5 * b + 2];
    p.wq[b] = (const char*)d_in[5 * b + 3]; p.wo[b] = (const char*)d_in[5 * b + 4];
  }
  p.out = (unsigned short*)d_out; p.ws = (char*)d_ws;
  (void)hipFuncSetAttribute((const void*)concat_heads_attn_kernel, hipFuncAttributeMaxDynamicSharedMemorySize, LDS_BYTES);
  hipLaunchKernelGGL(concat_heads_attn_kernel, dim3(NWG), dim3(NTHR), LDS_BYTES, stream, p);
}
```

```cpp
#include <hip/hip_runtime.h>
#include <hip/hip_bf16.h>
#include <stdint.h>

#define STAMPS 0
#define LDS_KB 84
#define WQ_WAIT 4
#define FEWN 8
#define SLEEPL 8
#define SLEEPN 24
#define STR_(x) #x
#define XSTR(x) STR_(x)

typedef unsigned u32;
typedef unsigned long long u64;
typedef u32 u32x4 __attribute__((ext_vector_type(4)));
typedef float f32x2 __attribute__((ext_vector_type(2)));
typedef float f32x4 __attribute__((ext_vector_type(4)));
typedef __bf16 bf16x8 __attribute__((ext_vector_type(8)));
typedef __bf16 bf16x2 __attribute__((ext_vector_type(2)));
#define GAS __attribute__((address_space(1)))
typedef GAS u64 gu64;

constexpr int D_MODEL = 512, HQ = 32, SK = 65536, DH = 128, WIN = 4096;
constexpr int NWG = 256, NTHR = 256;
constexpr int TILE_KEYS = 32, TILE_BYTES = TILE_KEYS * DH * 2;
constexpr int NPROD_A = 30, NPROD_B = 2;
constexpr float LOG2E = 1.4426950408889634f, SCALE = 0.08838834764831845f;
constexpr float THR = 8.0f / LOG2E;

constexpr size_t WS_GEN = 0;
constexpr size_t WS_Q = 4096;
constexpr size_t WS_H1D = WS_Q + (size_t)2 * 8 * 2 * 512 * 8;
constexpr size_t WS_H1M = WS_H1D + (size_t)2 * 8 * 16 * 32 * 32 * 8;
constexpr size_t WS_H2 = WS_H1M + (size_t)2 * 8 * 16 * 32 * 2 * 8;
constexpr size_t WS_END = WS_H2 + (size_t)2 * 32 * 128 * 16 * 8;
constexpr size_t WS_STAMP = 8u << 20;

constexpr int LDS_BYTES = LDS_KB * 1024;
constexpr u32 SPIN_CAP = 1u << 18;

struct Params {
  const char* x[2]; const char* wq[2]; const char* k[2]; const char* v[2]; char* ws; const char* wo[2];
  unsigned short* out;
};

__device__ __forceinline__ __amdgpu_buffer_rsrc_t mkrsrc(const void* p, int bytes) {
  return __builtin_amdgcn_make_buffer_rsrc((void*)p, 0, bytes, 0x00020000);
}
__device__ __forceinline__ u32x4 ld_nt(__amdgpu_buffer_rsrc_t r, u32 off) { return __builtin_amdgcn_raw_buffer_load_b128(r, off, 0, 2); }
__device__ __forceinline__ u32x4 ld_sc1(__amdgpu_buffer_rsrc_t r, u32 off) { return __builtin_amdgcn_raw_buffer_load_b128(r, off, 0, 16); }
__device__ __forceinline__ void st_sc1(__amdgpu_buffer_rsrc_t r, u32 off, u32x4 v) { __builtin_amdgcn_raw_buffer_store_b128(v, r, off, 0, 16); }
__device__ __forceinline__ float dot2(u32 a, u32 b, float c) {
  return __builtin_amdgcn_fdot2_f32_bf16(__builtin_bit_cast(bf16x2, a), __builtin_bit_cast(bf16x2, b), c, false);
}
__device__ __forceinline__ f32x4 mfma(u32x4 a, u32x4 b, f32x4 c) {
  return __builtin_amdgcn_mfma_f32_16x16x32_bf16(__builtin_bit_cast(bf16x8, a), __builtin_bit_cast(bf16x8, b), c, 0, 0, 0);
}
__device__ __forceinline__ u32 perm_lo(u32 hi, u32 lo) { return __builtin_amdgcn_perm(hi, lo, 0x05040100u); }
__device__ __forceinline__ u32 perm_hi(u32 hi, u32 lo) { return __builtin_amdgcn_perm(hi, lo, 0x07060302u); }
__device__ __forceinline__ u32x4 colfrag(const u32x4 (&r)[8], int t) {
  u32x4 a;
#pragma unroll
  for (int jj = 0; jj < 4; ++jj) a[jj] = (t & 1) ? perm_hi(r[2 * jj + 1][t >> 1], r[2 * jj][t >> 1]) : perm_lo(r[2 * jj + 1][t >> 1], r[2 * jj][t >> 1]);
  return a;
}
__device__ __forceinline__ u32 pack_bf16(float lo, float hi) { bf16x2 r = {(__bf16)lo, (__bf16)hi}; return __builtin_bit_cast(u32, r); }
__device__ __forceinline__ u32 cvtpk(float lo, float hi) { u32 r; asm("v_cvt_pk_bf16_f32 %0, %1, %2" : "=v"(r) : "v"(lo), "v"(hi)); return r; }
template <int CTRL> __device__ __forceinline__ float dpp(float x) {
  return __builtin_bit_cast(float, __builtin_amdgcn_mov_dpp(__builtin_bit_cast(int, x), CTRL, 0xf, 0xf, true));
}
constexpr int ROR2 = 0x122, ROR4 = 0x124, ROR8 = 0x128;
__device__ __forceinline__ float f(u32 x) { return __uint_as_float(x); }
__device__ __forceinline__ u32 u(float x) { return __float_as_uint(x); }
__device__ __forceinline__ float swap32_add(float a, float b) { auto s = __builtin_amdgcn_permlane32_swap(u(a), u(b), false, false); return f(s[0]) + f(s[1]); }
__device__ __forceinline__ float swap16_add(float a, float b) { auto s = __builtin_amdgcn_permlane16_swap(u(a), u(b), false, false); return f(s[0]) + f(s[1]); }
__device__ __forceinline__ float xrow_sum(float x) { x = swap16_add(x, x); return swap32_add(x, x); }
__device__ __forceinline__ float vmax0(float a, float b) { float r; asm("v_max_f32 %0, %1, %2" : "=v"(r) : "v"(a), "v"(b)); return r; }
__device__ __forceinline__ float vmax(float a, float b) { float r; asm("v_max_f32 %0, %1, %2\n\ts_nop 1" : "=v"(r) : "v"(a), "v"(b)); return r; }
__device__ __forceinline__ float xrow_max(float x) {
  { auto s = __builtin_amdgcn_permlane16_swap(u(x), u(x), false, false); x = vmax(f(s[0]), f(s[1])); }
  { auto s = __builtin_amdgcn_permlane32_swap(u(x), u(x), false, false); x = vmax(f(s[0]), f(s[1])); }
  return x;
}
#define DPP_MAX(NAME, CTRL) __device__ __forceinline__ float NAME(float x) { float r; \
  asm("s_nop 1\n\tv_max_f32_dpp %0, %1, %1 " CTRL " row_mask:0xf bank_mask:0xf\n\ts_nop 1" : "=&v"(r) : "v"(x)); return r; }
DPP_MAX(max_ror2, "row_ror:2") DPP_MAX(max_ror4, "row_ror:4") DPP_MAX(max_ror8, "row_ror:8")
__device__ __forceinline__ float ex2(float x) { return __builtin_amdgcn_exp2f(x); }
__device__ __forceinline__ void wg_barrier() { asm volatile("s_waitcnt lgkmcnt(0)\n\ts_barrier" ::: "memory"); }
__device__ __forceinline__ u32 enc24(float v, u32 tlo) { return ((u(v) + 0x80u) & 0xffffff00u) | tlo; }
__device__ __forceinline__ float dec24(u32 lo) { return f(lo & 0xffffff00u); }
__device__ __forceinline__ u32 bad40(const u32x4& g, u32 tlo, u32 thi) { return (((g[0] ^ tlo) | (g[2] ^ tlo)) & 0xffu) | (g[1] ^ thi) | (g[3] ^ thi); }
#define FENCE() __builtin_amdgcn_sched_barrier(0)
#define STAMP(i) do { } while (0)
#define LSTAMP(kk) do { } while (0)

__global__ void __launch_bounds__(NTHR, 2)
concat_heads_attn_kernel(Params p) {
  extern __shared__ __attribute__((aligned(16))) char smem[];
  constexpr int ACC_LD = 132;
  float* s_qred = (float*)smem;
  u32* s_q = (u32*)(smem + 2048);
  float* s_ml = (float*)(smem + 3072);
  float* s_acc = (float*)(smem + 4096);
  u32* s_o = (u32*)(smem + 40960);
  float* s_fin = (float*)(smem + 41216);
  u32* s_flag = (u32*)(smem + 41984);

  const int tid = threadIdx.x, lane = tid & 63;
  const int wave = __builtin_amdgcn_readfirstlane(tid >> 6);
  const int w = blockIdx.x, g = w & 7, i = w >> 3;
  STAMP(0);
  const int l15 = lane & 15, lq = lane >> 4;
  const int rb = i >> 4;

  u32x4 xw, wq[8];
  {
    const int row0 = 256 * ((i & 15) >> 3) + 64 * wave + 8 * (lane >> 3);
    const __amdgpu_buffer_rsrc_t rq = mkrsrc((rb ? p.wq[1] : p.wq[0]) + (size_t)(g * 512 + (i & 7) * 64) * 2, D_MODEL * HQ * DH * 2);
    const u32 off = (u32)row0 * (HQ * DH * 2) + (lane & 7) * 16;
#pragma unroll
    for (int n = 0; n < 8; ++n) wq[n] = ld_nt(rq, off + n * (HQ * DH * 2));
    xw = *(const GAS u32x4*)((rb ? p.x[1] : p.x[0]) + row0 * 2);
    FENCE();
  }
  u64 gen;
  asm volatile("s_load_dwordx2 %0, %1, 0x0" : "=s"(gen) : "s"(p.ws) : "memory");

  const int ab = i >= NPROD_A ? 1 : 0;
  const int pidx = ab ? i - NPROD_A : i;
  const int t0 = ab ? pidx * 64 : pidx * 68 + min(pidx, 8);
  const int nt = ab ? 64 : 68 + (pidx < 8 ? 1 : 0);
  const size_t kvoff = ((size_t)g * SK + (ab ? SK - WIN : 0) + (size_t)t0 * TILE_KEYS) * (DH * 2);
  const char* kp = (ab ? p.k[1] : p.k[0]) + kvoff;
  const char* vp = (ab ? p.v[1] : p.v[0]) + kvoff;
  const __amdgpu_buffer_rsrc_t rws = mkrsrc(p.ws, (int)WS_END);

  u32x4 K[2][8], V[2][8];
  const u32 koff0 = (4 * (l15 & 3) + (l15 >> 2)) * 256 + lq * 16, koff1 = koff0 + 4096;
  const u32 voff0 = lane * 16, voff1 = voff0 + 4096;
  auto issueK = [&](int r, int k, int half) {
    const int t = wave + 4 * k;
    const __amdgpu_buffer_rsrc_t rk = mkrsrc(kp + (size_t)t * TILE_BYTES, t < nt ? TILE_BYTES : 0);
    if (half != 1) {
#pragma unroll
      for (int j = 0; j < 4; ++j) K[r][j] = ld_nt(rk, koff0 + j * 64);
    }
    if (half != 0) {
#pragma unroll
      for (int j = 0; j < 4; ++j) K[r][4 + j] = ld_nt(rk, koff1 + j * 64);
    }
    FENCE();
  };
  auto issueV = [&](int r, int k) {
    const int t = wave + 4 * k;
    const __amdgpu_buffer_rsrc_t rv = mkrsrc(vp + (size_t)t * TILE_BYTES, t < nt ? TILE_BYTES : 0);
#pragma unroll
    for (int j = 0; j < 4; ++j) V[r][j] = ld_nt(rv, voff0 + j * 1024);
#pragma unroll
    for (int j = 0; j < 4; ++j) V[r][4 + j] = ld_nt(rv, voff1 + j * 1024);
    FENCE();
  };
  FENCE();
  asm volatile("s_waitcnt vmcnt(" XSTR(WQ_WAIT) ")" ::: "memory");
  FENCE();
  issueK(0, 0, 0);

  {
    float a[8];
#pragma unroll
    for (int c = 0; c < 8; ++c) a[c] = 0.f;
#pragma unroll
    for (int n = 0; n < 8; n += 2)
#pragma unroll
      for (int c = 0; c < 4; ++c) {
        a[2 * c] = dot2(perm_lo(wq[n + 1][c], wq[n][c]), xw[n >> 1], a[2 * c]);
        a[2 * c + 1] = dot2(perm_hi(wq[n + 1][c], wq[n][c]), xw[n >> 1], a[2 * c + 1]);
      }
    STAMP(1);
    float b[4];
#pragma unroll
    for (int c = 0; c < 4; ++c) b[c] = swap32_add(a[c], a[c + 4]);
    float y0 = swap16_add(b[0], b[2]), y1 = swap16_add(b[1], b[3]);
    y0 += dpp<ROR8>(y0); y1 += dpp<ROR8>(y1);
    if (l15 < 8) *(float2*)(s_qred + wave * 64 + 8 * l15 + 2 * lq) = make_float2(y0, y1);
  }
  wg_barrier();
  asm volatile("s_waitcnt lgkmcnt(0)" : "+s"(gen) :: "memory");
  const u64 tag = gen + 1;
  const u32 thi = (u32)(tag >> 8), tlo = (u32)tag & 0xffu;
  const u32 qtag = (u32)tag;
  if (tid < 32) {
    const float2 a0 = *(const float2*)(s_qred + 2 * lane), a1 = *(const float2*)(s_qred + 64 + 2 * lane);
    const float2 a2 = *(const float2*)(s_qred + 128 + 2 * lane), a3 = *(const float2*)(s_qred + 192 + 2 * lane);
    st_sc1(rws, (u32)WS_Q + (((rb * 8 + g) * 2 + ((i & 15) >> 3)) * 512 + (i & 7) * 64 + 2 * lane) * 8,
           u32x4{u((a0.x + a1.x) + (a2.x + a3.x)), qtag, u((a0.y + a1.y) + (a2.y + a3.y)), qtag});
  }
  STAMP(2);

  const u32 qoff = (u32)WS_Q + ((ab * 8 + g) * 2 * 512 + 2 * tid) * 8, qoff1 = qoff + 512 * 8;
  bool timeout = false, okw = false, have = false;
  u32x4 qa0 = {0u, 0u, 0u, 0u}, qa1 = qa0;
  const u32x4 srd_ws = {(u32)(uintptr_t)p.ws, (u32)((uintptr_t)p.ws >> 32) & 0xffffu, (u32)WS_END, 0x00020000u};
#define Q_POLL() do { \
    asm volatile("s_nop 4\n\tbuffer_load_dwordx4 %0, %2, %4, 0 offen sc1\n\tbuffer_load_dwordx4 %1, %3, %4, 0 offen sc1" \
                 : "+v"(qa0), "+v"(qa1) : "v"(qoff), "v"(qoff1), "s"(srd_ws) : "memory"); \
    FENCE(); } while (0)
#define Q_WAIT(N) asm volatile("s_waitcnt vmcnt(" #N ")" : "+v"(qa0), "+v"(qa1) :: "memory")
  auto q_extract = [&]() {
    const u32 bad = (qa0[1] ^ qtag) | (qa0[3] ^ qtag) | (qa1[1] ^ qtag) | (qa1[3] ^ qtag);
    if (__all(bad == 0)) {
      okw = true;
      s_q[tid] = pack_bf16(f(qa0[0]) + f(qa1[0]), f(qa0[2]) + f(qa1[2]));
    }
  };
  auto q_sync = [&](int it) {
    if (lane == 0) s_flag[(it & 1) * 4 + wave] = okw ? 1u : 0u;
    wg_barrier();
    const u32x4 fl = *(const u32x4*)(s_flag + (it & 1) * 4);
    have = __builtin_amdgcn_readfirstlane(fl[0] & fl[1] & fl[2] & fl[3]) != 0;
  };
  issueK(0, 0, 1); issueV(0, 0); issueK(1, 1, 2);
  asm volatile("s_barrier" ::: "memory");
  STAMP(10);
  Q_POLL();
  asm volatile("s_barrier" ::: "memory");
  issueV(1, 1);
  Q_WAIT(8);
  STAMP(11);
  q_extract();
  q_sync(0);
  for (u32 spins = 0; !have; ++spins) {
    if (spins > SPIN_CAP) { timeout = true; break; }
    __builtin_amdgcn_s_sleep(1);
    if (!okw) { Q_POLL(); Q_WAIT(0); q_extract(); }
    q_sync(spins + 1);
    STAMP(15);
  }
  u32x4 Q[4];
#pragma unroll
  for (int uu = 0; uu < 4; ++uu) Q[uu] = *(const u32x4*)(s_q + (l15 & 3) * 64 + uu * 16 + lq * 4);
  STAMP(3);

  const int kq = l15 >> 2;
  const bool hiT = kq >= 2, hiR = (kq & 1) != 0;
  float m = -INFINITY, mc = -INFINITY, mthr = -INFINITY, l = 0.f;
  f32x4 acc[8];
#pragma unroll
  for (int t = 0; t < 8; ++t) acc[t] = f32x4{0.f, 0.f, 0.f, 0.f};
  auto compute = [&](int r, int k) {
    if (wave + 4 * k < nt) {
      f32x4 s0 = {0.f, 0.f, 0.f, 0.f}, s1 = {0.f, 0.f, 0.f, 0.f};
#pragma unroll
      for (int uu = 0; uu < 4; ++uu) { s0 = mfma(K[r][uu], Q[uu], s0); s1 = mfma(K[r][4 + uu], Q[uu], s1); }
      FENCE();
      u32x4 A0 = colfrag(V[r], 0), A1 = colfrag(V[r], 1);
      asm volatile("" : "+v"(A0), "+v"(A1));
      FENCE();
      const float t0_ = hiT ? s1[0] : s0[0], t1_ = hiT ? s1[1] : s0[1], t2_ = hiT ? s1[2] : s0[2], t3_ = hiT ? s1[3] : s0[3];
      const float x0 = hiR ? t2_ : t0_, x1 = hiR ? t3_ : t1_;
      const u32 a = cvtpk(x0, x1);
      const f32x2 y = f32x2{f(a << 16), f(a & 0xffff0000u)} * SCALE;
      const u32 b = cvtpk(y[0], y[1]);
      const float z0 = f(b << 16), z1 = f(b & 0xffff0000u);
      if (__any(vmax0(z0, z1) > mthr)) {
        const float mn = vmax(m, xrow_max(vmax(z0, z1)));
        const float alpha = ex2((m - mn) * LOG2E);
        l *= alpha;
#pragma unroll
        for (int t = 0; t < 8; ++t) acc[t] *= alpha;
        m = mn; mc = mn * LOG2E; mthr = mn + THR;
      }
      const float p0 = ex2(fmaf(z0, LOG2E, -mc)), p1 = ex2(fmaf(z1, LOG2E, -mc));
      const u32 pp = pack_bf16(p0, p1);
      const u32x4 P = {kq == 0 ? pp : 0u, kq == 1 ? pp : 0u, kq == 2 ? pp : 0u, kq == 3 ? pp : 0u};
      l = dot2(pp, 0x3f803f80u, l);
      FENCE();
#pragma unroll
      for (int t = 0; t < 8; t += 2) {
        acc[t] = mfma(A0, P, acc[t]);
        FENCE();
        if (t + 2 < 8) A0 = colfrag(V[r], t + 2);
        FENCE();
        acc[t + 1] = mfma(A1, P, acc[t + 1]);
        FENCE();
        if (t + 3 < 8) A1 = colfrag(V[r], t + 3);
        FENCE();
      }
    }
    FENCE();
  };
#pragma nounroll
  for (int k = 0; k < 16; k += 2) {
    compute(0, k); LSTAMP(k); issueK(0, k + 2, 2); issueV(0, k + 2);
    compute(1, k + 1); LSTAMP(k + 1); issueK(1, k + 3, 2); issueV(1, k + 3);
  }
  compute(0, 16); LSTAMP(16);
  compute(1, 17); LSTAMP(17);
  STAMP(4);
  const int hh = (i & 15) >> 2, sl = i & 3;
  const int np = rb ? NPROD_B : NPROD_A;
  const int crole = (rb * 8 + g) * 16 + (i & 15);
  const int cpp = lane >> 1;
  const u32 om = (u32)WS_H1M + (crole * 32 + cpp) * 16;
  u32x4 wo[8];
  {
    const __amdgpu_buffer_rsrc_t ro = mkrsrc((rb ? p.wo[1] : p.wo[0]) + (size_t)((g * 4 + hh) * DH + 32 * sl) * (D_MODEL * 2), 32 * D_MODEL * 2);
#pragma unroll
    for (int n = 0; n < 8; ++n) {
      const int jj = 8 * lq + n;
      wo[n] = ld_nt(ro, (u32)(8 * (jj & 3) + (jj >> 2)) * (D_MODEL * 2) + (128 * wave + 8 * l15) * 2);
    }
    FENCE();
  }

  {
    const float M = max_ror8(max_ror4(m));
    const float fs = ex2((m - M) * LOG2E);
    l *= fs;
    l += dpp<ROR8>(l); l += dpp<ROR4>(l);
    l = xrow_sum(l);
    float* dst = s_acc + (wave * 16 + l15) * ACC_LD + 32 * lq;
#pragma unroll
    for (int t = 0; t < 8; ++t) *(f32x4*)(dst + 4 * t) = acc[t] * fs;
    if (lane < 4) *(float2*)(s_ml + (wave * 4 + lane) * 2) = make_float2(M * LOG2E, l);
  }
  wg_barrier();
  {
    const int h = wave;
    float mw[4], lw[4];
    float2 a[4][4];
#pragma unroll
    for (int ww = 0; ww < 4; ++ww) { const float2 t = *(const float2*)(s_ml + (ww * 4 + h) * 2); mw[ww] = t.x; lw[ww] = t.y; }
#pragma unroll
    for (int ww = 0; ww < 4; ++ww)
#pragma unroll
      for (int k = 0; k < 4; ++k) a[ww][k] = *(const float2*)(s_acc + (ww * 16 + 4 * k + h) * ACC_LD + 2 * lane);
    const float M = dec24(enc24(vmax0(vmax0(mw[0], mw[1]), vmax0(mw[2], mw[3])), 0));
    float o0 = 0.f, o1 = 0.f, L = 0.f;
#pragma unroll
    for (int ww = 0; ww < 4; ++ww) {
      const float fw = ex2(mw[ww] - M);
      o0 = fmaf(fw, (a[ww][0].x + a[ww][1].x) + (a[ww][2].x + a[ww][3].x), o0);
      o1 = fmaf(fw, (a[ww][0].y + a[ww][1].y) + (a[ww][2].y + a[ww][3].y), o1);
      L = fmaf(fw, lw[ww], L);
    }
    const int role = (ab * 8 + g) * 16 + h * 4 + lq;
    st_sc1(rws, (u32)WS_H1D + ((role * 32 + pidx) * 32 + 2 * l15) * 8, u32x4{enc24(o0, tlo), thi, enc24(o1, tlo), thi});
    if (l15 == 0) st_sc1(rws, (u32)WS_H1M + (role * 32 + pidx) * 16, u32x4{enc24(M, tlo), thi, enc24(L, tlo), thi});
  }
  STAMP(5);

  u32x4 ml = ld_sc1(rws, cpp < np ? om : 0x7ffffff0u);
  FENCE();
  u32x4 B[8];
#pragma unroll
  for (int t = 0; t < 8; ++t) B[t] = colfrag(wo, t);
  {
    asm volatile("" : "+v"(B[0]), "+v"(B[1]), "+v"(B[2]), "+v"(B[3]), "+v"(B[4]), "+v"(B[5]), "+v"(B[6]), "+v"(B[7]));
    const u32 od = (u32)WS_H1D + ((crole * 32 + cpp) * 32 + 8 * wave + 4 * (lane & 1)) * 8;
    bool dmiss = cpp < np, mlmiss = dmiss && bad40(ml, tlo, thi) != 0;
    u32x4 d0 = {0u, 0u, 0u, 0u}, d1 = d0;
    for (u32 spins = 0;; ++spins) {
      const int nm = __popcll(__ballot(mlmiss));
      if (spins) {
        if (timeout || spins > SPIN_CAP) { timeout = true; break; }
        if (nm > SLEEPN) __builtin_amdgcn_s_sleep(SLEEPL); else if (nm) __builtin_amdgcn_s_sleep(1);
      }
      const bool getd = dmiss && (!mlmiss || nm <= FEWN);
      if (mlmiss) ml = ld_sc1(rws, om);
      if (getd) { d0 = ld_sc1(rws, od); d1 = ld_sc1(rws, od + 16); }
      if (mlmiss) mlmiss = bad40(ml, tlo, thi) != 0;
      if (getd) dmiss = (bad40(d0, tlo, thi) | bad40(d1, tlo, thi)) != 0;
      if (__ballot(mlmiss | dmiss) == 0) break;
    }
    STAMP(6);
    const float mp = cpp < np ? dec24(ml[0]) : -INFINITY;
    const float M = xrow_max(max_ror8(max_ror4(max_ror2(mp))));
    const float fw = ex2(mp - M);
    float L = fw * dec24(ml[2]);
    const float v0 = fw * dec24(d0[0]), v1 = fw * dec24(d0[2]), v2 = fw * dec24(d1[0]), v3 = fw * dec24(d1[2]);
    L += dpp<ROR2>(L); L += dpp<ROR4>(L); L += dpp<ROR8>(L); L = xrow_sum(L);
    const float y0 = swap32_add(v0, v2), y1 = swap32_add(v1, v3);
    float z = swap16_add(y0, y1);
    z += dpp<ROR2>(z); z += dpp<ROR4>(z); z += dpp<ROR8>(z);
    z *= __builtin_amdgcn_rcpf(L);
    if (l15 < 2) ((unsigned short*)s_o)[8 * wave + 4 * l15 + lq] = (unsigned short)(pack_bf16(z, 0.f) & 0xffffu);
  }
  wg_barrier();
  {
    const u32x4 op = *(const u32x4*)(s_o + 4 * lq);
    f32x4 d[8];
#pragma unroll
    for (int t = 0; t < 8; ++t) d[t] = mfma(op, B[t], f32x4{0.f, 0.f, 0.f, 0.f});
    const float y0 = lq == 0 ? d[0][0] : lq == 1 ? d[2][0] : lq == 2 ? d[4][0] : d[6][0];
    const float y1 = lq == 0 ? d[1][0] : lq == 1 ? d[3][0] : lq == 2 ? d[5][0] : d[7][0];
    const int col = 128 * wave + 8 * l15 + 2 * lq;
    const int prod = g * 16 + (i & 15);
    st_sc1(rws, (u32)WS_H2 + (((rb * 32 + (col >> 4)) * 128 + prod) * 16 + (col & 15)) * 8, u32x4{enc24(y0, tlo), thi, enc24(y1, tlo), thi});
  }
  STAMP(7);

  if (w < 64) {
    const int fb = w >> 5, fc = w & 31;
    const int cc = tid & 7, pp = tid >> 3;
    const u32 o2 = (u32)WS_H2 + (((fb * 32 + fc) * 128 + pp) * 16 + 2 * cc) * 8;
    u32x4 d[4];
    bool miss[4] = {true, true, true, true};
#pragma unroll
    for (int k = 0; k < 4; ++k) d[k] = u32x4{0u, 0u, 0u, 0u};
    for (u32 spins = 0;; ++spins) {
#pragma unroll
      for (int k = 0; k < 4; ++k) if (miss[k]) d[k] = ld_sc1(rws, o2 + k * (32 * 128));
#pragma unroll
      for (int k = 0; k < 4; ++k) miss[k] = bad40(d[k], tlo, thi) != 0;
      const int nm = __popcll(__ballot(miss[0])) + __popcll(__ballot(miss[1])) + __popcll(__ballot(miss[2])) + __popcll(__ballot(miss[3]));
      if (nm == 0) break;
      if (timeout || spins > SPIN_CAP) { timeout = true; break; }
      if (nm > 4 * SLEEPN) __builtin_amdgcn_s_sleep(SLEEPL); else __builtin_amdgcn_s_sleep(1);
    }
    STAMP(8);
    const float y0 = (dec24(d[0][0]) + dec24(d[1][0])) + (dec24(d[2][0]) + dec24(d[3][0]));
    const float y1 = (dec24(d[0][2]) + dec24(d[1][2])) + (dec24(d[2][2]) + dec24(d[3][2]));
    float z = swap32_add(y0, y1);
    z = swap16_add(z, z);
    z += dpp<ROR8>(z);
    if ((lane & 31) < 8) s_fin[wave * 16 + 2 * cc + (lane >> 5)] = z;
    wg_barrier();
    if (tid < 8) {
      const float r0 = (s_fin[2 * tid] + s_fin[16 + 2 * tid]) + (s_fin[32 + 2 * tid] + s_fin[48 + 2 * tid]);
      const float r1 = (s_fin[2 * tid + 1] + s_fin[16 + 2 * tid + 1]) + (s_fin[32 + 2 * tid + 1] + s_fin[48 + 2 * tid + 1]);
      u32 o = pack_bf16(r0, r1);
      if (timeout) o = 0x7fc07fc0u;
      *(GAS u32*)((char*)p.out + (fb * 512 + fc * 16 + 2 * tid) * 2) = o;
    }
    if (w == 0 && wave == 0) {
      for (u32 sp = 0;; ++sp) {
        const u32x4 e0 = ld_sc1(rws, (u32)WS_H2 + ((32 * 128 + lane) * 16) * 8), e1 = ld_sc1(rws, (u32)WS_H2 + ((32 * 128 + 64 + lane) * 16) * 8);
        if (__all((bad40(e0, tlo, thi) | bad40(e1, tlo, thi)) == 0)) break;
        if (timeout || sp > SPIN_CAP) break;
        __builtin_amdgcn_s_sleep(1);
      }
      if (lane == 0) __hip_atomic_store((gu64*)(p.ws + WS_GEN), tag, __ATOMIC_RELAXED, __HIP_MEMORY_SCOPE_AGENT);
    }
  }
  STAMP(9);
}

extern "C" void kernel_launch(void* const* d_in, const int* in_sizes, int n_in, void* d_out, int out_size, void* d_ws, size_t ws_size,
                              hipStream_t stream) {
  (void)in_sizes; (void)n_in; (void)out_size; (void)ws_size;
  Params p;
  for (int b = 0; b < 2; ++b) {
    p.x[b] = (const char*)d_in[5 * b + 0]; p.k[b] = (const char*)d_in[5 * b + 1]; p.v[b] = (const char*)d_in[5 * b + 2];
    p.wq[b] = (const char*)d_in[5 * b + 3]; p.wo[b] = (const char*)d_in[5 * b + 4];
  }
  p.out = (unsigned short*)d_out; p.ws = (char*)d_ws;
  (void)hipFuncSetAttribute((const void*)concat_heads_attn_kernel, hipFuncAttributeMaxDynamicSharedMemorySize, LDS_BYTES);
  hipLaunchKernelGGL(concat_heads_attn_kernel, dim3(NWG), dim3(NTHR), LDS_BYTES, stream, p);
}
```

```cpp
#include <hip/hip_runtime.h>
#include <hip/hip_bf16.h>
#include <stdint.h>

#define STAMPS 0
#define LDS_KB 84
#define WQ_WAIT 4
#define FEWN 8
#define SLEEPL 8
#define SLEEPN 24
#define STR_(x) #x
#define XSTR(x) STR_(x)

typedef unsigned u32;
typedef unsigned long long u64;
typedef u32 u32x4 __attribute__((ext_vector_type(4)));
typedef float f32x2 __attribute__((ext_vector_type(2)));
typedef float f32x4 __attribute__((ext_vector_type(4)));
typedef __bf16 bf16x8 __attribute__((ext_vector_type(8)));
typedef __bf16 bf16x2 __attribute__((ext_vector_type(2)));
#define GAS __attribute__((address_space(1)))
typedef GAS u64 gu64;

constexpr int D_MODEL = 512, HQ = 32, SK = 65536, DH = 128, WIN = 4096;
constexpr int NWG = 256, NTHR = 256;
constexpr int TILE_KEYS = 32, TILE_BYTES = TILE_KEYS * DH * 2;
constexpr int NPROD_A = 30, NPROD_B = 2;
constexpr float LOG2E = 1.4426950408889634f, SCALE = 0.08838834764831845f;
constexpr float THR = 8.0f / LOG2E;

constexpr size_t WS_GEN = 0;
constexpr size_t WS_Q = 4096;
constexpr size_t WS_H1D = WS_Q + (size_t)2 * 8 * 2 * 512 * 8;
constexpr size_t WS_H1M = WS_H1D + (size_t)2 * 8 * 16 * 32 * 32 * 8;
constexpr size_t WS_H2 = WS_H1M + (size_t)2 * 8 * 16 * 32 * 2 * 8;
constexpr size_t WS_END = WS_H2 + (size_t)2 * 32 * 128 * 16 * 8;
constexpr size_t WS_STAMP = 8u << 20;

constexpr int LDS_BYTES = LDS_KB * 1024;
constexpr u32 SPIN_CAP = 1u << 18;

struct Params {
  const char* x[2]; const char* wq[2]; const char* k[2]; const char* v[2]; char* ws; const char* wo[2];
  unsigned short* out;
};

__device__ __forceinline__ __amdgpu_buffer_rsrc_t mkrsrc(const void* p, int bytes) {
  return __builtin_amdgcn_make_buffer_rsrc((void*)p, 0, bytes, 0x00020000);
}
__device__ __forceinline__ u32x4 ld_nt(__amdgpu_buffer_rsrc_t r, u32 off) { return __builtin_amdgcn_raw_buffer_load_b128(r, off, 0, 2); }
__device__ __forceinline__ u32x4 ld_sc1(__amdgpu_buffer_rsrc_t r, u32 off) { return __builtin_amdgcn_raw_buffer_load_b128(r, off, 0, 16); }
__device__ __forceinline__ void st_sc1(__amdgpu_buffer_rsrc_t r, u32 off, u32x4 v) { __builtin_amdgcn_raw_buffer_store_b128(v, r, off, 0, 16); }
__device__ __forceinline__ float dot2(u32 a, u32 b, float c) {
  return __builtin_amdgcn_fdot2_f32_bf16(__builtin_bit_cast(bf16x2, a), __builtin_bit_cast(bf16x2, b), c, false);
}
__device__ __forceinline__ f32x4 mfma(u32x4 a, u32x4 b, f32x4 c) {
  return __builtin_amdgcn_mfma_f32_16x16x32_bf16(__builtin_bit_cast(bf16x8, a), __builtin_bit_cast(bf16x8, b), c, 0, 0, 0);
}
__device__ __forceinline__ u32 perm_lo(u32 hi, u32 lo) { return __builtin_amdgcn_perm(hi, lo, 0x05040100u); }
__device__ __forceinline__ u32 perm_hi(u32 hi, u32 lo) { return __builtin_amdgcn_perm(hi, lo, 0x07060302u); }
__device__ __forceinline__ u32x4 colfrag(const u32x4 (&r)[8], int t) {
  u32x4 a;
#pragma unroll
  for (int jj = 0; jj < 4; ++jj) a[jj] = (t & 1) ? perm_hi(r[2 * jj + 1][t >> 1], r[2 * jj][t >> 1]) : perm_lo(r[2 * jj + 1][t >> 1], r[2 * jj][t >> 1]);
  return a;
}
__device__ __forceinline__ u32 pack_bf16(float lo, float hi) { bf16x2 r = {(__bf16)lo, (__bf16)hi}; return __builtin_bit_cast(u32, r); }
__device__ __forceinline__ u32 cvtpk(float lo, float hi) { u32 r; asm("v_cvt_pk_bf16_f32 %0, %1, %2" : "=v"(r) : "v"(lo), "v"(hi)); return r; }
template <int CTRL> __device__ __forceinline__ float dpp(float x) {
  return __builtin_bit_cast(float, __builtin_amdgcn_mov_dpp(__builtin_bit_cast(int, x), CTRL, 0xf, 0xf, true));
}
constexpr int ROR2 = 0x122, ROR4 = 0x124, ROR8 = 0x128;
__device__ __forceinline__ float f(u32 x) { return __uint_as_float(x); }
__device__ __forceinline__ u32 u(float x) { return __float_as_uint(x); }
__device__ __forceinline__ float swap32_add(float a, float b) { auto s = __builtin_amdgcn_permlane32_swap(u(a), u(b), false, false); return f(s[0]) + f(s[1]); }
__device__ __forceinline__ float swap16_add(float a, float b) { auto s = __builtin_amdgcn_permlane16_swap(u(a), u(b), false, false); return f(s[0]) + f(s[1]); }
__device__ __forceinline__ float xrow_sum(float x) { x = swap16_add(x, x); return swap32_add(x, x); }
__device__ __forceinline__ float vmax0(float a, float b) { float r; asm("v_max_f32 %0, %1, %2" : "=v"(r) : "v"(a), "v"(b)); return r; }
__device__ __forceinline__ float vmax(float a, float b) { float r; asm("v_max_f32 %0, %1, %2\n\ts_nop 1" : "=v"(r) : "v"(a), "v"(b)); return r; }
__device__ __forceinline__ float xrow_max(float x) {
  { auto s = __builtin_amdgcn_permlane16_swap(u(x), u(x), false, false); x = vmax(f(s[0]), f(s[1])); }
  { auto s = __builtin_amdgcn_permlane32_swap(u(x), u(x), false, false); x = vmax(f(s[0]), f(s[1])); }
  return x;
}
#define DPP_MAX(NAME, CTRL) __device__ __forceinline__ float NAME(float x) { float r; \
  asm("s_nop 1\n\tv_max_f32_dpp %0, %1, %1 " CTRL " row_mask:0xf bank_mask:0xf\n\ts_nop 1" : "=&v"(r) : "v"(x)); return r; }
DPP_MAX(max_ror2, "row_ror:2") DPP_MAX(max_ror4, "row_ror:4") DPP_MAX(max_ror8, "row_ror:8")
__device__ __forceinline__ float ex2(float x) { return __builtin_amdgcn_exp2f(x); }
__device__ __forceinline__ void wg_barrier() { asm volatile("s_waitcnt lgkmcnt(0)\n\ts_barrier" ::: "memory"); }
__device__ __forceinline__ u32 enc24(float v, u32 tlo) { return ((u(v) + 0x80u) & 0xffffff00u) | tlo; }
__device__ __forceinline__ float dec24(u32 lo) { return f(lo & 0xffffff00u); }
__device__ __forceinline__ u32 bad40(const u32x4& g, u32 tlo, u32 thi) { return (((g[0] ^ tlo) | (g[2] ^ tlo)) & 0xffu) | (g[1] ^ thi) | (g[3] ^ thi); }
#define FENCE() __builtin_amdgcn_sched_barrier(0)
#define STAMP(i) do { } while (0)
#define LSTAMP(kk) do { } while (0)

__global__ void __launch_bounds__(NTHR, 2)
concat_heads_attn_kernel(Params p) {
  extern __shared__ __attribute__((aligned(16))) char smem[];
  constexpr int ACC_LD = 132;
  float* s_qred = (float*)smem;
  u32* s_q = (u32*)(smem + 2048);
  float* s_ml = (float*)(smem + 3072);
  float* s_acc = (float*)(smem + 4096);
  u32* s_o = (u32*)(smem + 40960);
  float* s_fin = (float*)(smem + 41216);
  u32* s_flag = (u32*)(smem + 41984);

  const int tid = threadIdx.x, lane = tid & 63;
  const int wave = __builtin_amdgcn_readfirstlane(tid >> 6);
  const int w = blockIdx.x, g = w & 7, i = w >> 3;
  STAMP(0);
  const int l15 = lane & 15, lq = lane >> 4;
  const int rb = i >> 4;

  u32x4 xw, wq[8];
  {
    const int row0 = 256 * ((i & 15) >> 3) + 64 * wave + 8 * (lane >> 3);
    const __amdgpu_buffer_rsrc_t rq = mkrsrc((rb ? p.wq[1] : p.wq[0]) + (size_t)(g * 512 + (i & 7) * 64) * 2, D_MODEL * HQ * DH * 2);
    const u32 off = (u32)row0 * (HQ * DH * 2) + (lane & 7) * 16;
#pragma unroll
    for (int n = 0; n < 8; ++n) wq[n] = ld_nt(rq, off + n * (HQ * DH * 2));
    xw = *(const GAS u32x4*)((rb ? p.x[1] : p.x[0]) + row0 * 2);
    FENCE();
  }
  u64 gen;
  asm volatile("s_load_dwordx2 %0, %1, 0x0" : "=s"(gen) : "s"(p.ws) : "memory");

  const int ab = i >= NPROD_A ? 1 : 0;
  const int pidx = ab ? i - NPROD_A : i;
  const int t0 = ab ? pidx * 64 : pidx * 68 + min(pidx, 8);
  const int nt = ab ? 64 : 68 + (pidx < 8 ? 1 : 0);
  const size_t kvoff = ((size_t)g * SK + (ab ? SK - WIN : 0) + (size_t)t0 * TILE_KEYS) * (DH * 2);
  const char* kp = (ab ? p.k[1] : p.k[0]) + kvoff;
  const char* vp = (ab ? p.v[1] : p.v[0]) + kvoff;
  const __amdgpu_buffer_rsrc_t rws = mkrsrc(p.ws, (int)WS_END);

  u32x4 K[2][8], V[2][8];
  const u32 koff0 = (4 * (l15 & 3) + (l15 >> 2)) * 256 + lq * 16, koff1 = koff0 + 4096;
  const u32 voff0 = lane * 16, voff1 = voff0 + 4096;
  auto issueK = [&](int r, int k, int half) {
    const int t = wave + 4 * k;
    const __amdgpu_buffer_rsrc_t rk = mkrsrc(kp + (size_t)t * TILE_BYTES, t < nt ? TILE_BYTES : 0);
    if (half != 1) {
#pragma unroll
      for (int j = 0; j < 4; ++j) K[r][j] = ld_nt(rk, koff0 + j * 64);
    }
    if (half != 0) {
#pragma unroll
      for (int j = 0; j < 4; ++j) K[r][4 + j] = ld_nt(rk, koff1 + j * 64);
    }
    FENCE();
  };
  auto issueV = [&](int r, int k) {
    const int t = wave + 4 * k;
    const __amdgpu_buffer_rsrc_t rv = mkrsrc(vp + (size_t)t * TILE_BYTES, t < nt ? TILE_BYTES : 0);
#pragma unroll
    for (int j = 0; j < 4; ++j) V[r][j] = ld_nt(rv, voff0 + j * 1024);
#pragma unroll
    for (int j = 0; j < 4; ++j) V[r][4 + j] = ld_nt(rv, voff1 + j * 1024);
    FENCE();
  };
  FENCE();
  asm volatile("s_waitcnt vmcnt(" XSTR(WQ_WAIT) ")" ::: "memory");
  FENCE();
  issueK(0, 0, 0);

  {
    float a[8];
#pragma unroll
    for (int c = 0; c < 8; ++c) a[c] = 0.f;
#pragma unroll
    for (int n = 0; n < 8; n += 2)
#pragma unroll
      for (int c = 0; c < 4; ++c) {
        a[2 * c] = dot2(perm_lo(wq[n + 1][c], wq[n][c]), xw[n >> 1], a[2 * c]);
        a[2 * c + 1] = dot2(perm_hi(wq[n + 1][c], wq[n][c]), xw[n >> 1], a[2 * c + 1]);
      }
    STAMP(1);
    float b[4];
#pragma unroll
    for (int c = 0; c < 4; ++c) b[c] = swap32_add(a[c], a[c + 4]);
    float y0 = swap16_add(b[0], b[2]), y1 = swap16_add(b[1], b[3]);
    y0 += dpp<ROR8>(y0); y1 += dpp<ROR8>(y1);
    if (l15 < 8) *(float2*)(s_qred + wave * 64 + 8 * l15 + 2 * lq) = make_float2(y0, y1);
  }
  wg_barrier();
  asm volatile("s_waitcnt lgkmcnt(0)" : "+s"(gen) :: "memory");
  const u64 tag = gen + 1;
  const u32 thi = (u32)(tag >> 8), tlo = (u32)tag & 0xffu;
  const u32 qtag = (u32)tag;
  if (tid < 32) {
    const float2 a0 = *(const float2*)(s_qred + 2 * lane), a1 = *(const float2*)(s_qred + 64 + 2 * lane);
    const float2 a2 = *(const float2*)(s_qred + 128 + 2 * lane), a3 = *(const float2*)(s_qred + 192 + 2 * lane);
    st_sc1(rws, (u32)WS_Q + (((rb * 8 + g) * 2 + ((i & 15) >> 3)) * 512 + (i & 7) * 64 + 2 * lane) * 8,
           u32x4{u((a0.x + a1.x) + (a2.x + a3.x)), qtag, u((a0.y + a1.y) + (a2.y + a3.y)), qtag});
  }
  STAMP(2);

  const u32 qoff = (u32)WS_Q + ((ab * 8 + g) * 2 * 512 + 2 * tid) * 8, qoff1 = qoff + 512 * 8;
  bool timeout = false, okw = false, have = false;
  u32x4 qa0 = {0u, 0u, 0u, 0u}, qa1 = qa0;
  const u32x4 srd_ws = {(u32)(uintptr_t)p.ws, (u32)((uintptr_t)p.ws >> 32) & 0xffffu, (u32)WS_END, 0x00020000u};
#define Q_POLL() do { \
    asm volatile("s_nop 4\n\tbuffer_load_dwordx4 %0, %2, %4, 0 offen sc1\n\tbuffer_load_dwordx4 %1, %3, %4, 0 offen sc1" \
                 : "+v"(qa0), "+v"(qa1) : "v"(qoff), "v"(qoff1), "s"(srd_ws) : "memory"); \
    FENCE(); } while (0)
#define Q_WAIT(N) asm volatile("s_waitcnt vmcnt(" #N ")" : "+v"(qa0), "+v"(qa1) :: "memory")
  auto q_extract = [&]() {
    const u32 bad = (qa0[1] ^ qtag) | (qa0[3] ^ qtag) | (qa1[1] ^ qtag) | (qa1[3] ^ qtag);
    if (__all(bad == 0)) {
      okw = true;
      s_q[tid] = pack_bf16(f(qa0[0]) + f(qa1[0]), f(qa0[2]) + f(qa1[2]));
    }
  };
  auto q_sync = [&](int it) {
    if (lane == 0) s_flag[(it & 1) * 4 + wave] = okw ? 1u : 0u;
    wg_barrier();
    const u32x4 fl = *(const u32x4*)(s_flag + (it & 1) * 4);
    have = __builtin_amdgcn_readfirstlane(fl[0] & fl[1] & fl[2] & fl[3]) != 0;
  };
  issueK(0, 0, 1); issueV(0, 0); issueK(1, 1, 2);
  asm volatile("s_barrier" ::: "memory");
  STAMP(10);
  Q_POLL();
  asm volatile("s_barrier" ::: "memory");
  issueV(1, 1);
  Q_WAIT(8);
  STAMP(11);
  q_extract();
  q_sync(0);
  for (u32 spins = 0; !have; ++spins) {
    if (spins > SPIN_CAP) { timeout = true; break; }
    __builtin_amdgcn_s_sleep(1);
    if (!okw) { Q_POLL(); Q_WAIT(0); q_extract(); }
    q_sync(spins + 1);
    STAMP(15);
  }
  u32x4 Q[4];
#pragma unroll
  for (int uu = 0; uu < 4; ++uu) Q[uu] = *(const u32x4*)(s_q + (l15 & 3) * 64 + uu * 16 + lq * 4);
  STAMP(3);

  const int kq = l15 >> 2;
  const bool hiT = kq >= 2, hiR = (kq & 1) != 0;
  float m = -INFINITY, mc = -INFINITY, mthr = -INFINITY, l = 0.f;
  f32x4 acc[8];
#pragma unroll
  for (int t = 0; t < 8; ++t) acc[t] = f32x4{0.f, 0.f, 0.f, 0.f};
  auto compute = [&](int r, int k) {
    if (wave + 4 * k < nt) {
      f32x4 s0 = {0.f, 0.f, 0.f, 0.f}, s1 = {0.f, 0.f, 0.f, 0.f};
#pragma unroll
      for (int uu = 0; uu < 4; ++uu) { s0 = mfma(K[r][uu], Q[uu], s0); s1 = mfma(K[r][4 + uu], Q[uu], s1); }
      FENCE();
      u32x4 A0 = colfrag(V[r], 0), A1 = colfrag(V[r], 1);
      asm volatile("" : "+v"(A0), "+v"(A1));
      FENCE();
      const float t0_ = hiT ? s1[0] : s0[0], t1_ = hiT ? s1[1] : s0[1], t2_ = hiT ? s1[2] : s0[2], t3_ = hiT ? s1[3] : s0[3];
      const float x0 = hiR ? t2_ : t0_, x1 = hiR ? t3_ : t1_;
      const u32 a = cvtpk(x0, x1);
      const f32x2 y = f32x2{f(a << 16), f(a & 0xffff0000u)} * SCALE;
      const u32 b = cvtpk(y[0], y[1]);
      const float z0 = f(b << 16), z1 = f(b & 0xffff0000u);
      if (__any(vmax0(z0, z1) > mthr)) {
        const float mn = vmax(m, xrow_max(vmax(z0, z1)));
        const float alpha = ex2((m - mn) * LOG2E);
        l *= alpha;
#pragma unroll
        for (int t = 0; t < 8; ++t) acc[t] *= alpha;
        m = mn; mc = mn * LOG2E; mthr = mn + THR;
      }
      const float p0 = ex2(fmaf(z0, LOG2E, -mc)), p1 = ex2(fmaf(z1, LOG2E, -mc));
      const u32 pp = pack_bf16(p0, p1);
      const u32x4 P = {kq == 0 ? pp : 0u, kq == 1 ? pp : 0u, kq == 2 ? pp : 0u, kq == 3 ? pp : 0u};
      l = dot2(pp, 0x3f803f80u, l);
      FENCE();
#pragma unroll
      for (int t = 0; t < 8; t += 2) {
        acc[t] = mfma(A0, P, acc[t]);
        FENCE();
        if (t + 2 < 8) A0 = colfrag(V[r], t + 2);
        FENCE();
        acc[t + 1] = mfma(A1, P, acc[t + 1]);
        FENCE();
        if (t + 3 < 8) A1 = colfrag(V[r], t + 3);
        FENCE();
      }
    }
    FENCE();
  };
#pragma nounroll
  for (int k = 0; k < 16; k += 2) {
    compute(0, k); LSTAMP(k); issueK(0, k + 2, 2); issueV(0, k + 2);
    compute(1, k + 1); LSTAMP(k + 1); issueK(1, k + 3, 2); issueV(1, k + 3);
  }
  compute(0, 16); LSTAMP(16);
  compute(1, 17); LSTAMP(17);
  STAMP(4);
  const int hh = (i & 15) >> 2, sl = i & 3;
  const int np = rb ? NPROD_B : NPROD_A;
  const int crole = (rb * 8 + g) * 16 + (i & 15);
  const int cpp = lane >> 1;
  const u32 om = (u32)WS_H1M + (crole * 32 + cpp) * 16;
  u32x4 wo[8];
  {
    const __amdgpu_buffer_rsrc_t ro = mkrsrc((rb ? p.wo[1] : p.wo[0]) + (size_t)((g * 4 + hh) * DH + 32 * sl) * (D_MODEL * 2), 32 * D_MODEL * 2);
#pragma unroll
    for (int n = 0; n < 8; ++n) {
      const int jj = 8 * lq + n;
      wo[n] = ld_nt(ro, (u32)(8 * (jj & 3) + (jj >> 2)) * (D_MODEL * 2) + (128 * wave + 8 * l15) * 2);
    }
    FENCE();
  }

  {
    const float M = max_ror8(max_ror4(m));
    const float fs = ex2((m - M) * LOG2E);
    l *= fs;
    l += dpp<ROR8>(l); l += dpp<ROR4>(l);
    l = xrow_sum(l);
    float* dst = s_acc + (wave * 16 + l15) * ACC_LD + 32 * lq;
#pragma unroll
    for (int t = 0; t < 8; ++t) *(f32x4*)(dst + 4 * t) = acc[t] * fs;
    if (lane < 4) *(float2*)(s_ml + (wave * 4 + lane) * 2) = make_float2(M * LOG2E, l);
  }
  wg_barrier();
  {
    const int h = wave;
    float mw[4], lw[4];
    float2 a[4][4];
#pragma unroll
    for (int ww = 0; ww < 4; ++ww) { const float2 t = *(const float2*)(s_ml + (ww * 4 + h) * 2); mw[ww] = t.x; lw[ww] = t.y; }
#pragma unroll
    for (int ww = 0; ww < 4; ++ww)
#pragma unroll
      for (int k = 0; k < 4; ++k) a[ww][k] = *(const float2*)(s_acc + (ww * 16 + 4 * k + h) * ACC_LD + 2 * lane);
    const float M = dec24(enc24(vmax0(vmax0(mw[0], mw[1]), vmax0(mw[2], mw[3])), 0));
    float o0 = 0.f, o1 = 0.f, L = 0.f;
#pragma unroll
    for (int ww = 0; ww < 4; ++ww) {
      const float fw = ex2(mw[ww] - M);
      o0 = fmaf(fw, (a[ww][0].x + a[ww][1].x) + (a[ww][2].x + a[ww][3].x), o0);
      o1 = fmaf(fw, (a[ww][0].y + a[ww][1].y) + (a[ww][2].y + a[ww][3].y), o1);
      L = fmaf(fw, lw[ww], L);
    }
    const int role = (ab * 8 + g) * 16 + h * 4 + lq;
    st_sc1(rws, (u32)WS_H1D + ((role * 32 + pidx) * 32 + 2 * l15) * 8, u32x4{enc24(o0, tlo), thi, enc24(o1, tlo), thi});
    if (l15 == 0) st_sc1(rws, (u32)WS_H1M + (role * 32 + pidx) * 16, u32x4{enc24(M, tlo), thi, enc24(L, tlo), thi});
  }
  if (w == NWG - 1 && wave == 3) {
    for (u32 sp = 0;; ++sp) {
      u32 bad = 0;
#pragma unroll
      for (int r = 0; r < 4; ++r) { const u32x4 e = ld_sc1(rws, (u32)WS_Q + (lane + 64 * r) * (64 * 8)); bad |= e[1] ^ qtag; }
      if (__all(bad == 0)) break;
      if (timeout || sp > SPIN_CAP) break;
      __builtin_amdgcn_s_sleep(1);
    }
    if (lane == 0) __hip_atomic_store((gu64*)(p.ws + WS_GEN), tag, __ATOMIC_RELAXED, __HIP_MEMORY_SCOPE_AGENT);
  }
  STAMP(5);

  u32x4 ml = ld_sc1(rws, cpp < np ? om : 0x7ffffff0u);
  FENCE();
  u32x4 B[8];
#pragma unroll
  for (int t = 0; t < 8; ++t) B[t] = colfrag(wo, t);
  {
    asm volatile("" : "+v"(B[0]), "+v"(B[1]), "+v"(B[2]), "+v"(B[3]), "+v"(B[4]), "+v"(B[5]), "+v"(B[6]), "+v"(B[7]));
    const u32 od = (u32)WS_H1D + ((crole * 32 + cpp) * 32 + 8 * wave + 4 * (lane & 1)) * 8;
    bool dmiss = cpp < np, mlmiss = dmiss && bad40(ml, tlo, thi) != 0;
    u32x4 d0 = {0u, 0u, 0u, 0u}, d1 = d0;
    for (u32 spins = 0;; ++spins) {
      const int nm = __popcll(__ballot(mlmiss));
      if (spins) {
        if (timeout || spins > SPIN_CAP) { timeout = true; break; }
        if (nm > SLEEPN) __builtin_amdgcn_s_sleep(SLEEPL); else if (nm) __builtin_amdgcn_s_sleep(1);
      }
      const bool getd = dmiss && (!mlmiss || nm <= FEWN);
      if (mlmiss) ml = ld_sc1(rws, om);
      if (getd) { d0 = ld_sc1(rws, od); d1 = ld_sc1(rws, od + 16); }
      if (mlmiss) mlmiss = bad40(ml, tlo, thi) != 0;
      if (getd) dmiss = (bad40(d0, tlo, thi) | bad40(d1, tlo, thi)) != 0;
      if (__ballot(mlmiss | dmiss) == 0) break;
    }
    STAMP(6);
    const float mp = cpp < np ? dec24(ml[0]) : -INFINITY;
    const float M = xrow_max(max_ror8(max_ror4(max_ror2(mp))));
    const float fw = ex2(mp - M);
    float L = fw * dec24(ml[2]);
    const float v0 = fw * dec24(d0[0]), v1 = fw * dec24(d0[2]), v2 = fw * dec24(d1[0]), v3 = fw * dec24(d1[2]);
    L += dpp<ROR2>(L); L += dpp<ROR4>(L); L += dpp<ROR8>(L); L = xrow_sum(L);
    const float y0 = swap32_add(v0, v2), y1 = swap32_add(v1, v3);
    float z = swap16_add(y0, y1);
    z += dpp<ROR2>(z); z += dpp<ROR4>(z); z += dpp<ROR8>(z);
    z *= __builtin_amdgcn_rcpf(L);
    if (l15 < 2) ((unsigned short*)s_o)[8 * wave + 4 * l15 + lq] = (unsigned short)(pack_bf16(z, 0.f) & 0xffffu);
  }
  wg_barrier();
  {
    const u32x4 op = *(const u32x4*)(s_o + 4 * lq);
    f32x4 d[8];
#pragma unroll
    for (int t = 0; t < 8; ++t) d[t] = mfma(op, B[t], f32x4{0.f, 0.f, 0.f, 0.f});
    const float y0 = lq == 0 ? d[0][0] : lq == 1 ? d[2][0] : lq == 2 ? d[4][0] : d[6][0];
    const float y1 = lq == 0 ? d[1][0] : lq == 1 ? d[3][0] : lq == 2 ? d[5][0] : d[7][0];
    const int col = 128 * wave + 8 * l15 + 2 * lq;
    const int prod = g * 16 + (i & 15);
    st_sc1(rws, (u32)WS_H2 + (((rb * 32 + (col >> 4)) * 128 + prod) * 16 + (col & 15)) * 8, u32x4{enc24(y0, tlo), thi, enc24(y1, tlo), thi});
  }
  STAMP(7);

  if (w < 64) {
    const int fb = w >> 5, fc = w & 31;
    const int cc = tid & 7, pp = tid >> 3;
    const u32 o2 = (u32)WS_H2 + (((fb * 32 + fc) * 128 + pp) * 16 + 2 * cc) * 8;
    u32x4 d[4];
    bool miss[4] = {true, true, true, true};
#pragma unroll
    for (int k = 0; k < 4; ++k) d[k] = u32x4{0u, 0u, 0u, 0u};
    for (u32 spins = 0;; ++spins) {
#pragma unroll
      for (int k = 0; k < 4; ++k) if (miss[k]) d[k] = ld_sc1(rws, o2 + k * (32 * 128));
#pragma unroll
      for (int k = 0; k < 4; ++k) miss[k] = bad40(d[k], tlo, thi) != 0;
      const int nm = __popcll(__ballot(miss[0])) + __popcll(__ballot(miss[1])) + __popcll(__ballot(miss[2])) + __popcll(__ballot(miss[3]));
      if (nm == 0) break;
      if (timeout || spins > SPIN_CAP) { timeout = true; break; }
      if (nm > 4 * SLEEPN) __builtin_amdgcn_s_sleep(SLEEPL); else __builtin_amdgcn_s_sleep(1);
    }
    STAMP(8);
    const float y0 = (dec24(d[0][0]) + dec24(d[1][0])) + (dec24(d[2][0]) + dec24(d[3][0]));
    const float y1 = (dec24(d[0][2]) + dec24(d[1][2])) + (dec24(d[2][2]) + dec24(d[3][2]));
    float z = swap32_add(y0, y1);
    z = swap16_add(z, z);
    z += dpp<ROR8>(z);
    if ((lane & 31) < 8) s_fin[wave * 16 + 2 * cc + (lane >> 5)] = z;
    wg_barrier();
    if (tid < 8) {
      const float r0 = (s_fin[2 * tid] + s_fin[16 + 2 * tid]) + (s_fin[32 + 2 * tid] + s_fin[48 + 2 * tid]);
      const float r1 = (s_fin[2 * tid + 1] + s_fin[16 + 2 * tid + 1]) + (s_fin[32 + 2 * tid + 1] + s_fin[48 + 2 * tid + 1]);
      u32 o = pack_bf16(r0, r1);
      if (timeout) o = 0x7fc07fc0u;
      *(GAS u32*)((char*)p.out + (fb * 512 + fc * 16 + 2 * tid) * 2) = o;
    }
  }
  STAMP(9);
}

extern "C" void kernel_launch(void* const* d_in, const int* in_sizes, int n_in, void* d_out, int out_size, void* d_ws, size_t ws_size,
                              hipStream_t stream) {
  (void)in_sizes; (void)n_in; (void)out_size; (void)ws_size;
  Params p;
  for (int b = 0; b < 2; ++b) {
    p.x[b] = (const char*)d_in[5 * b + 0]; p.k[b] = (const char*)d_in[5 * b + 1]; p.v[b] = (const char*)d_in[5 * b + 2];
    p.wq[b] = (const char*)d_in[5 * b + 3]; p.wo[b] = (const char*)d_in[5 * b + 4];
  }
  p.out = (unsigned short*)d_out; p.ws = (char*)d_ws;
  (void)hipFuncSetAttribute((const void*)concat_heads_attn_kernel, hipFuncAttributeMaxDynamicSharedMemorySize, LDS_BYTES);
  hipLaunchKernelGGL(concat_heads_attn_kernel, dim3(NWG), dim3(NTHR), LDS_BYTES, stream, p);
}
```

```cpp
#include <hip/hip_runtime.h>
#include <hip/hip_bf16.h>
#include <stdint.h>


typedef unsigned u32;
typedef unsigned long long u64;
typedef u32 u32x4 __attribute__((ext_vector_type(4)));
typedef float f32x2 __attribute__((ext_vector_type(2)));
typedef float f32x4 __attribute__((ext_vector_type(4)));
typedef __bf16 bf16x8 __attribute__((ext_vector_type(8)));
typedef __bf16 bf16x2 __attribute__((ext_vector_type(2)));
#define GAS __attribute__((address_space(1)))
typedef GAS u64 gu64;

constexpr int D_MODEL = 512, HQ = 32, SK = 65536, DH = 128, WIN = 4096;
constexpr int NWG = 256, NTHR = 256;
constexpr int TILE_KEYS = 32, TILE_BYTES = TILE_KEYS * DH * 2;
constexpr int NPROD_A = 30, NPROD_B = 2;
constexpr float LOG2E = 1.4426950408889634f, SCALE = 0.08838834764831845f;
constexpr float THR = 8.0f / LOG2E;

constexpr size_t WS_GEN = 0;
constexpr size_t WS_Q = 4096;
constexpr size_t WS_H1D = WS_Q + (size_t)2 * 8 * 2 * 512 * 8;
constexpr size_t WS_H1M = WS_H1D + (size_t)2 * 8 * 16 * 32 * 32 * 8;
constexpr size_t WS_H2 = WS_H1M + (size_t)2 * 8 * 16 * 32 * 2 * 8;
constexpr size_t WS_END = WS_H2 + (size_t)2 * 32 * 128 * 16 * 8;

constexpr int LDS_BYTES = 84 * 1024;
constexpr int FEWN = 8;
constexpr int SLEEPN = 24, SLEEPL = 8;
constexpr u32 SPIN_CAP = 1u << 18;

struct Params {
  const char* x[2]; const char* wq[2]; const char* k[2]; const char* v[2]; char* ws; const char* wo[2];
  unsigned short* out;
};

__device__ __forceinline__ __amdgpu_buffer_rsrc_t mkrsrc(const void* p, int bytes) {
  return __builtin_amdgcn_make_buffer_rsrc((void*)p, 0, bytes, 0x00020000);
}
__device__ __forceinline__ u32x4 ld_nt(__amdgpu_buffer_rsrc_t r, u32 off) { return __builtin_amdgcn_raw_buffer_load_b128(r, off, 0, 2); }
__device__ __forceinline__ u32x4 ld_sc1(__amdgpu_buffer_rsrc_t r, u32 off) { return __builtin_amdgcn_raw_buffer_load_b128(r, off, 0, 16); }
__device__ __forceinline__ void st_sc1(__amdgpu_buffer_rsrc_t r, u32 off, u32x4 v) { __builtin_amdgcn_raw_buffer_store_b128(v, r, off, 0, 16); }
__device__ __forceinline__ float dot2(u32 a, u32 b, float c) {
  return __builtin_amdgcn_fdot2_f32_bf16(__builtin_bit_cast(bf16x2, a), __builtin_bit_cast(bf16x2, b), c, false);
}
__device__ __forceinline__ f32x4 mfma(u32x4 a, u32x4 b, f32x4 c) {
  return __builtin_amdgcn_mfma_f32_16x16x32_bf16(__builtin_bit_cast(bf16x8, a), __builtin_bit_cast(bf16x8, b), c, 0, 0, 0);
}
__device__ __forceinline__ u32 perm_lo(u32 hi, u32 lo) { return __builtin_amdgcn_perm(hi, lo, 0x05040100u); }
__device__ __forceinline__ u32 perm_hi(u32 hi, u32 lo) { return __builtin_amdgcn_perm(hi, lo, 0x07060302u); }
__device__ __forceinline__ u32x4 colfrag(const u32x4 (&r)[8], int t) {
  u32x4 a;
#pragma unroll
  for (int jj = 0; jj < 4; ++jj) a[jj] = (t & 1) ? perm_hi(r[2 * jj + 1][t >> 1], r[2 * jj][t >> 1]) : perm_lo(r[2 * jj + 1][t >> 1], r[2 * jj][t >> 1]);
  return a;
}
__device__ __forceinline__ u32 pack_bf16(float lo, float hi) { bf16x2 r = {(__bf16)lo, (__bf16)hi}; return __builtin_bit_cast(u32, r); }
__device__ __forceinline__ u32 cvtpk(float lo, float hi) { u32 r; asm("v_cvt_pk_bf16_f32 %0, %1, %2" : "=v"(r) : "v"(lo), "v"(hi)); return r; }
template <int CTRL> __device__ __forceinline__ float dpp(float x) {
  return __builtin_bit_cast(float, __builtin_amdgcn_mov_dpp(__builtin_bit_cast(int, x), CTRL, 0xf, 0xf, true));
}
constexpr int ROR2 = 0x122, ROR4 = 0x124, ROR8 = 0x128;
__device__ __forceinline__ float f(u32 x) { return __uint_as_float(x); }
__device__ __forceinline__ u32 u(float x) { return __float_as_uint(x); }
__device__ __forceinline__ float swap32_add(float a, float b) { auto s = __builtin_amdgcn_permlane32_swap(u(a), u(b), false, false); return f(s[0]) + f(s[1]); }
__device__ __forceinline__ float swap16_add(float a, float b) { auto s = __builtin_amdgcn_permlane16_swap(u(a), u(b), false, false); return f(s[0]) + f(s[1]); }
__device__ __forceinline__ float xrow_sum(float x) { x = swap16_add(x, x); return swap32_add(x, x); }
__device__ __forceinline__ float vmax0(float a, float b) { float r; asm("v_max_f32 %0, %1, %2" : "=v"(r) : "v"(a), "v"(b)); return r; }
__device__ __forceinline__ float vmax(float a, float b) { float r; asm("v_max_f32 %0, %1, %2\n\ts_nop 1" : "=v"(r) : "v"(a), "v"(b)); return r; }
__device__ __forceinline__ float xrow_max(float x) {
  { auto s = __builtin_amdgcn_permlane16_swap(u(x), u(x), false, false); x = vmax(f(s[0]), f(s[1])); }
  { auto s = __builtin_amdgcn_permlane32_swap(u(x), u(x), false, false); x = vmax(f(s[0]), f(s[1])); }
  return x;
}
#define DPP_MAX(NAME, CTRL) __device__ __forceinline__ float NAME(float x) { float r; \
  asm("s_nop 1\n\tv_max_f32_dpp %0, %1, %1 " CTRL " row_mask:0xf bank_mask:0xf\n\ts_nop 1" : "=&v"(r) : "v"(x)); return r; }
DPP_MAX(max_ror2, "row_ror:2") DPP_MAX(max_ror4, "row_ror:4") DPP_MAX(max_ror8, "row_ror:8")
__device__ __forceinline__ float ex2(float x) { return __builtin_amdgcn_exp2f(x); }
__device__ __forceinline__ void wg_barrier() { asm volatile("s_waitcnt lgkmcnt(0)\n\ts_barrier" ::: "memory"); }
__device__ __forceinline__ u32 enc24(float v, u32 tlo) { return ((u(v) + 0x80u) & 0xffffff00u) | tlo; }
__device__ __forceinline__ float dec24(u32 lo) { return f(lo & 0xffffff00u); }
__device__ __forceinline__ u32 bad40(const u32x4& g, u32 tlo, u32 thi) { return (((g[0] ^ tlo) | (g[2] ^ tlo)) & 0xffu) | (g[1] ^ thi) | (g[3] ^ thi); }
#define FENCE() __builtin_amdgcn_sched_barrier(0)

__global__ void __launch_bounds__(NTHR, 2)
concat_heads_attn_kernel(Params p) {
  extern __shared__ __attribute__((aligned(16))) char smem[];
  constexpr int ACC_LD = 132;
  float* s_qred = (float*)smem;
  u32* s_q = (u32*)(smem + 2048);
  float* s_ml = (float*)(smem + 3072);
  float* s_acc = (float*)(smem + 4096);
  u32* s_o = (u32*)(smem + 40960);
  float* s_fin = (float*)(smem + 41216);
  u32* s_flag = (u32*)(smem + 41984);

  const int tid = threadIdx.x, lane = tid & 63;
  const int wave = __builtin_amdgcn_readfirstlane(tid >> 6);
  const int w = blockIdx.x, g = w & 7, i = w >> 3;
  const int l15 = lane & 15, lq = lane >> 4;
  const int rb = i >> 4;

  u32x4 xw, wq[8];
  {
    const int row0 = 256 * ((i & 15) >> 3) + 64 * wave + 8 * (lane >> 3);
    const __amdgpu_buffer_rsrc_t rq = mkrsrc((rb ? p.wq[1] : p.wq[0]) + (size_t)(g * 512 + (i & 7) * 64) * 2, D_MODEL * HQ * DH * 2);
    const u32 off = (u32)row0 * (HQ * DH * 2) + (lane & 7) * 16;
#pragma unroll
    for (int n = 0; n < 8; ++n) wq[n] = ld_nt(rq, off + n * (HQ * DH * 2));
    xw = *(const GAS u32x4*)((rb ? p.x[1] : p.x[0]) + row0 * 2);
    FENCE();
  }
  u64 gen;
  asm volatile("s_load_dwordx2 %0, %1, 0x0" : "=s"(gen) : "s"(p.ws) : "memory");

  const int ab = i >= NPROD_A ? 1 : 0;
  const int pidx = ab ? i - NPROD_A : i;
  const int t0 = ab ? pidx * 64 : pidx * 68 + min(pidx, 8);
  const int nt = ab ? 64 : 68 + (pidx < 8 ? 1 : 0);
  const size_t kvoff = ((size_t)g * SK + (ab ? SK - WIN : 0) + (size_t)t0 * TILE_KEYS) * (DH * 2);
  const char* kp = (ab ? p.k[1] : p.k[0]) + kvoff;
  const char* vp = (ab ? p.v[1] : p.v[0]) + kvoff;
  const __amdgpu_buffer_rsrc_t rws = mkrsrc(p.ws, (int)WS_END);

  u32x4 K[2][8], V[2][8];
  const u32 koff0 = (4 * (l15 & 3) + (l15 >> 2)) * 256 + lq * 16, koff1 = koff0 + 4096;
  const u32 voff0 = lane * 16, voff1 = voff0 + 4096;
  auto issueK = [&](int r, int k, int half) {
    const int t = wave + 4 * k;
    const __amdgpu_buffer_rsrc_t rk = mkrsrc(kp + (size_t)t * TILE_BYTES, t < nt ? TILE_BYTES : 0);
    if (half != 1) {
#pragma unroll
      for (int j = 0; j < 4; ++j) K[r][j] = ld_nt(rk, koff0 + j * 64);
    }
    if (half != 0) {
#pragma unroll
      for (int j = 0; j < 4; ++j) K[r][4 + j] = ld_nt(rk, koff1 + j * 64);
    }
    FENCE();
  };
  auto issueV = [&](int r, int k) {
    const int t = wave + 4 * k;
    const __amdgpu_buffer_rsrc_t rv = mkrsrc(vp + (size_t)t * TILE_BYTES, t < nt ? TILE_BYTES : 0);
#pragma unroll
    for (int j = 0; j < 4; ++j) V[r][j] = ld_nt(rv, voff0 + j * 1024);
#pragma unroll
    for (int j = 0; j < 4; ++j) V[r][4 + j] = ld_nt(rv, voff1 + j * 1024);
    FENCE();
  };
  FENCE();
  asm volatile("s_waitcnt vmcnt(4)" ::: "memory");
  FENCE();
  issueK(0, 0, 0);

  {
    float a[8];
#pragma unroll
    for (int c = 0; c < 8; ++c) a[c] = 0.f;
#pragma unroll
    for (int n = 0; n < 8; n += 2)
#pragma unroll
      for (int c = 0; c < 4; ++c) {
        a[2 * c] = dot2(perm_lo(wq[n + 1][c], wq[n][c]), xw[n >> 1], a[2 * c]);
        a[2 * c + 1] = dot2(perm_hi(wq[n + 1][c], wq[n][c]), xw[n >> 1], a[2 * c + 1]);
      }
    float b[4];
#pragma unroll
    for (int c = 0; c < 4; ++c) b[c] = swap32_add(a[c], a[c + 4]);
    float y0 = swap16_add(b[0], b[2]), y1 = swap16_add(b[1], b[3]);
    y0 += dpp<ROR8>(y0); y1 += dpp<ROR8>(y1);
    if (l15 < 8) *(float2*)(s_qred + wave * 64 + 8 * l15 + 2 * lq) = make_float2(y0, y1);
  }
  wg_barrier();
  asm volatile("s_waitcnt lgkmcnt(0)" : "+s"(gen) :: "memory");
  const u64 tag = gen + 1;
  const u32 thi = (u32)(tag >> 8), tlo = (u32)tag & 0xffu;
  const u32 qtag = (u32)tag;
  if (tid < 32) {
    const float2 a0 = *(const float2*)(s_qred + 2 * lane), a1 = *(const float2*)(s_qred + 64 + 2 * lane);
    const float2 a2 = *(const float2*)(s_qred + 128 + 2 * lane), a3 = *(const float2*)(s_qred + 192 + 2 * lane);
    st_sc1(rws, (u32)WS_Q + (((rb * 8 + g) * 2 + ((i & 15) >> 3)) * 512 + (i & 7) * 64 + 2 * lane) * 8,
           u32x4{u((a0.x + a1.x) + (a2.x + a3.x)), qtag, u((a0.y + a1.y) + (a2.y + a3.y)), qtag});
  }

  const u32 qoff = (u32)WS_Q + ((ab * 8 + g) * 2 * 512 + 2 * tid) * 8, qoff1 = qoff + 512 * 8;
  bool timeout = false, okw = false, have = false;
  u32x4 qa0 = {0u, 0u, 0u, 0u}, qa1 = qa0;
  const u32x4 srd_ws = {(u32)(uintptr_t)p.ws, (u32)((uintptr_t)p.ws >> 32) & 0xffffu, (u32)WS_END, 0x00020000u};
#define Q_POLL() do { \
    asm volatile("s_nop 4\n\tbuffer_load_dwordx4 %0, %2, %4, 0 offen sc1\n\tbuffer_load_dwordx4 %1, %3, %4, 0 offen sc1" \
                 : "+v"(qa0), "+v"(qa1) : "v"(qoff), "v"(qoff1), "s"(srd_ws) : "memory"); \
    FENCE(); } while (0)
#define Q_WAIT(N) asm volatile("s_waitcnt vmcnt(" #N ")" : "+v"(qa0), "+v"(qa1) :: "memory")
  auto q_extract = [&]() {
    const u32 bad = (qa0[1] ^ qtag) | (qa0[3] ^ qtag) | (qa1[1] ^ qtag) | (qa1[3] ^ qtag);
    if (__all(bad == 0)) {
      okw = true;
      s_q[tid] = pack_bf16(f(qa0[0]) + f(qa1[0]), f(qa0[2]) + f(qa1[2]));
    }
  };
  auto q_sync = [&](int it) {
    if (lane == 0) s_flag[(it & 1) * 4 + wave] = okw ? 1u : 0u;
    wg_barrier();
    const u32x4 fl = *(const u32x4*)(s_flag + (it & 1) * 4);
    have = __builtin_amdgcn_readfirstlane(fl[0] & fl[1] & fl[2] & fl[3]) != 0;
  };
  issueK(0, 0, 1); issueV(0, 0); issueK(1, 1, 2);
  asm volatile("s_barrier" ::: "memory");
  Q_POLL();
  asm volatile("s_barrier" ::: "memory");
  issueV(1, 1);
  Q_WAIT(8);
  q_extract();
  q_sync(0);
  for (u32 spins = 0; !have; ++spins) {
    if (spins > SPIN_CAP) { timeout = true; break; }
    __builtin_amdgcn_s_sleep(1);
    if (!okw) { Q_POLL(); Q_WAIT(0); q_extract(); }
    q_sync(spins + 1);
  }
  u32x4 Q[4];
#pragma unroll
  for (int uu = 0; uu < 4; ++uu) Q[uu] = *(const u32x4*)(s_q + (l15 & 3) * 64 + uu * 16 + lq * 4);

  const int kq = l15 >> 2;
  const bool hiT = kq >= 2, hiR = (kq & 1) != 0;
  float m = -INFINITY, mc = -INFINITY, mthr = -INFINITY, l = 0.f;
  f32x4 acc[8];
#pragma unroll
  for (int t = 0; t < 8; ++t) acc[t] = f32x4{0.f, 0.f, 0.f, 0.f};
  auto compute = [&](int r, int k) {
    if (wave + 4 * k < nt) {
      f32x4 s0 = {0.f, 0.f, 0.f, 0.f}, s1 = {0.f, 0.f, 0.f, 0.f};
#pragma unroll
      for (int uu = 0; uu < 4; ++uu) { s0 = mfma(K[r][uu], Q[uu], s0); s1 = mfma(K[r][4 + uu], Q[uu], s1); }
      FENCE();
      u32x4 A0 = colfrag(V[r], 0), A1 = colfrag(V[r], 1);
      asm volatile("" : "+v"(A0), "+v"(A1));
      FENCE();
      const float t0_ = hiT ? s1[0] : s0[0], t1_ = hiT ? s1[1] : s0[1], t2_ = hiT ? s1[2] : s0[2], t3_ = hiT ? s1[3] : s0[3];
      const float x0 = hiR ? t2_ : t0_, x1 = hiR ? t3_ : t1_;
      const u32 a = cvtpk(x0, x1);
      const f32x2 y = f32x2{f(a << 16), f(a & 0xffff0000u)} * SCALE;
      const u32 b = cvtpk(y[0], y[1]);
      const float z0 = f(b << 16), z1 = f(b & 0xffff0000u);
      if (__any(vmax0(z0, z1) > mthr)) {
        const float mn = vmax(m, xrow_max(vmax(z0, z1)));
        const float alpha = ex2((m - mn) * LOG2E);
        l *= alpha;
#pragma unroll
        for (int t = 0; t < 8; ++t) acc[t] *= alpha;
        m = mn; mc = mn * LOG2E; mthr = mn + THR;
      }
      const float p0 = ex2(fmaf(z0, LOG2E, -mc)), p1 = ex2(fmaf(z1, LOG2E, -mc));
      const u32 pp = pack_bf16(p0, p1);
      const u32x4 P = {kq == 0 ? pp : 0u, kq == 1 ? pp : 0u, kq == 2 ? pp : 0u, kq == 3 ? pp : 0u};
      l = dot2(pp, 0x3f803f80u, l);
      FENCE();
#pragma unroll
      for (int t = 0; t < 8; t += 2) {
        acc[t] = mfma(A0, P, acc[t]);
        FENCE();
        if (t + 2 < 8) A0 = colfrag(V[r], t + 2);
        FENCE();
        acc[t + 1] = mfma(A1, P, acc[t + 1]);
        FENCE();
        if (t + 3 < 8) A1 = colfrag(V[r], t + 3);
        FENCE();
      }
    }
    FENCE();
  };
#pragma nounroll
  for (int k = 0; k < 16; k += 2) {
    compute(0, k); issueK(0, k + 2, 2); issueV(0, k + 2);
    compute(1, k + 1); issueK(1, k + 3, 2); issueV(1, k + 3);
  }
  compute(0, 16);
  compute(1, 17);
  const int hh = (i & 15) >> 2, sl = i & 3;
  const int np = rb ? NPROD_B : NPROD_A;
  const int crole = (rb * 8 + g) * 16 + (i & 15);
  const int cpp = lane >> 1;
  const u32 om = (u32)WS_H1M + (crole * 32 + cpp) * 16;
  u32x4 wo[8];
  {
    const __amdgpu_buffer_rsrc_t ro = mkrsrc((rb ? p.wo[1] : p.wo[0]) + (size_t)((g * 4 + hh) * DH + 32 * sl) * (D_MODEL * 2), 32 * D_MODEL * 2);
#pragma unroll
    for (int n = 0; n < 8; ++n) {
      const int jj = 8 * lq + n;
      wo[n] = ld_nt(ro, (u32)(8 * (jj & 3) + (jj >> 2)) * (D_MODEL * 2) + (128 * wave + 8 * l15) * 2);
    }
    FENCE();
  }

  {
    const float M = max_ror8(max_ror4(m));
    const float fs = ex2((m - M) * LOG2E);
    l *= fs;
    l += dpp<ROR8>(l); l += dpp<ROR4>(l);
    l = xrow_sum(l);
    float* dst = s_acc + (wave * 16 + l15) * ACC_LD + 32 * lq;
#pragma unroll
    for (int t = 0; t < 8; ++t) *(f32x4*)(dst + 4 * t) = acc[t] * fs;
    if (lane < 4) *(float2*)(s_ml + (wave * 4 + lane) * 2) = make_float2(M * LOG2E, l);
  }
  wg_barrier();
  {
    const int h = wave;
    float mw[4], lw[4];
    float2 a[4][4];
#pragma unroll
    for (int ww = 0; ww < 4; ++ww) { const float2 t = *(const float2*)(s_ml + (ww * 4 + h) * 2); mw[ww] = t.x; lw[ww] = t.y; }
#pragma unroll
    for (int ww = 0; ww < 4; ++ww)
#pragma unroll
      for (int k = 0; k < 4; ++k) a[ww][k] = *(const float2*)(s_acc + (ww * 16 + 4 * k + h) * ACC_LD + 2 * lane);
    const float M = dec24(enc24(vmax0(vmax0(mw[0], mw[1]), vmax0(mw[2], mw[3])), 0));
    float o0 = 0.f, o1 = 0.f, L = 0.f;
#pragma unroll
    for (int ww = 0; ww < 4; ++ww) {
      const float fw = ex2(mw[ww] - M);
      o0 = fmaf(fw, (a[ww][0].x + a[ww][1].x) + (a[ww][2].x + a[ww][3].x), o0);
      o1 = fmaf(fw, (a[ww][0].y + a[ww][1].y) + (a[ww][2].y + a[ww][3].y), o1);
      L = fmaf(fw, lw[ww], L);
    }
    const int role = (ab * 8 + g) * 16 + h * 4 + lq;
    st_sc1(rws, (u32)WS_H1D + ((role * 32 + pidx) * 32 + 2 * l15) * 8, u32x4{enc24(o0, tlo), thi, enc24(o1, tlo), thi});
    if (l15 == 0) st_sc1(rws, (u32)WS_H1M + (role * 32 + pidx) * 16, u32x4{enc24(M, tlo), thi, enc24(L, tlo), thi});
  }
  if (w == NWG - 1 && wave == 3) {
    for (u32 sp = 0;; ++sp) {
      u32 bad = 0;
#pragma unroll
      for (int r = 0; r < 4; ++r) { const u32x4 e = ld_sc1(rws, (u32)WS_Q + (lane + 64 * r) * (64 * 8)); bad |= e[1] ^ qtag; }
      if (__all(bad == 0)) break;
      if (timeout || sp > SPIN_CAP) break;
      __builtin_amdgcn_s_sleep(1);
    }
    if (lane == 0) __hip_atomic_store((gu64*)(p.ws + WS_GEN), tag, __ATOMIC_RELAXED, __HIP_MEMORY_SCOPE_AGENT);
  }

  u32x4 ml = ld_sc1(rws, cpp < np ? om : 0x7ffffff0u);
  FENCE();
  u32x4 B[8];
#pragma unroll
  for (int t = 0; t < 8; ++t) B[t] = colfrag(wo, t);
  {
    asm volatile("" : "+v"(B[0]), "+v"(B[1]), "+v"(B[2]), "+v"(B[3]), "+v"(B[4]), "+v"(B[5]), "+v"(B[6]), "+v"(B[7]));
    const u32 od = (u32)WS_H1D + ((crole * 32 + cpp) * 32 + 8 * wave + 4 * (lane & 1)) * 8;
    bool dmiss = cpp < np, mlmiss = dmiss && bad40(ml, tlo, thi) != 0;
    u32x4 d0 = {0u, 0u, 0u, 0u}, d1 = d0;
    for (u32 spins = 0;; ++spins) {
      const int nm = __popcll(__ballot(mlmiss));
      if (spins) {
        if (timeout || spins > SPIN_CAP) { timeout = true; break; }
        if (nm > SLEEPN) __builtin_amdgcn_s_sleep(SLEEPL); else if (nm) __builtin_amdgcn_s_sleep(1);
      }
      const bool getd = dmiss && (!mlmiss || nm <= FEWN);
      if (mlmiss) ml = ld_sc1(rws, om);
      if (getd) { d0 = ld_sc1(rws, od); d1 = ld_sc1(rws, od + 16); }
      if (mlmiss) mlmiss = bad40(ml, tlo, thi) != 0;
      if (getd) dmiss = (bad40(d0, tlo, thi) | bad40(d1, tlo, thi)) != 0;
      if (__ballot(mlmiss | dmiss) == 0) break;
    }
    const float mp = cpp < np ? dec24(ml[0]) : -INFINITY;
    const float M = xrow_max(max_ror8(max_ror4(max_ror2(mp))));
    const float fw = ex2(mp - M);
    float L = fw * dec24(ml[2]);
    const float v0 = fw * dec24(d0[0]), v1 = fw * dec24(d0[2]), v2 = fw * dec24(d1[0]), v3 = fw * dec24(d1[2]);
    L += dpp<ROR2>(L); L += dpp<ROR4>(L); L += dpp<ROR8>(L); L = xrow_sum(L);
    const float y0 = swap32_add(v0, v2), y1 = swap32_add(v1, v3);
    float z = swap16_add(y0, y1);
    z += dpp<ROR2>(z); z += dpp<ROR4>(z); z += dpp<ROR8>(z);
    z *= __builtin_amdgcn_rcpf(L);
    if (l15 < 2) ((unsigned short*)s_o)[8 * wave + 4 * l15 + lq] = (unsigned short)(pack_bf16(z, 0.f) & 0xffffu);
  }
  wg_barrier();
  {
    const u32x4 op = *(const u32x4*)(s_o + 4 * lq);
    f32x4 d[8];
#pragma unroll
    for (int t = 0; t < 8; ++t) d[t] = mfma(op, B[t], f32x4{0.f, 0.f, 0.f, 0.f});
    const float y0 = lq == 0 ? d[0][0] : lq == 1 ? d[2][0] : lq == 2 ? d[4][0] : d[6][0];
    const float y1 = lq == 0 ? d[1][0] : lq == 1 ? d[3][0] : lq == 2 ? d[5][0] : d[7][0];
    const int col = 128 * wave + 8 * l15 + 2 * lq;
    const int prod = g * 16 + (i & 15);
    st_sc1(rws, (u32)WS_H2 + (((rb * 32 + (col >> 4)) * 128 + prod) * 16 + (col & 15)) * 8, u32x4{enc24(y0, tlo), thi, enc24(y1, tlo), thi});
  }

  if (w < 64) {
    const int fb = w >> 5, fc = w & 31;
    const int cc = tid & 7, pp = tid >> 3;
    const u32 o2 = (u32)WS_H2 + (((fb * 32 + fc) * 128 + pp) * 16 + 2 * cc) * 8;
    u32x4 d[4];
    bool miss[4] = {true, true, true, true};
#pragma unroll
    for (int k = 0; k < 4; ++k) d[k] = u32x4{0u, 0u, 0u, 0u};
    for (u32 spins = 0;; ++spins) {
#pragma unroll
      for (int k = 0; k < 4; ++k) if (miss[k]) d[k] = ld_sc1(rws, o2 + k * (32 * 128));
#pragma unroll
      for (int k = 0; k < 4; ++k) miss[k] = bad40(d[k], tlo, thi) != 0;
      const int nm = __popcll(__ballot(miss[0])) + __popcll(__ballot(miss[1])) + __popcll(__ballot(miss[2])) + __popcll(__ballot(miss[3]));
      if (nm == 0) break;
      if (timeout || spins > SPIN_CAP) { timeout = true; break; }
      if (nm > 4 * SLEEPN) __builtin_amdgcn_s_sleep(SLEEPL); else __builtin_amdgcn_s_sleep(1);
    }
    const float y0 = (dec24(d[0][0]) + dec24(d[1][0])) + (dec24(d[2][0]) + dec24(d[3][0]));
    const float y1 = (dec24(d[0][2]) + dec24(d[1][2])) + (dec24(d[2][2]) + dec24(d[3][2]));
    float z = swap32_add(y0, y1);
    z = swap16_add(z, z);
    z += dpp<ROR8>(z);
    if ((lane & 31) < 8) s_fin[wave * 16 + 2 * cc + (lane >> 5)] = z;
    wg_barrier();
    if (tid < 8) {
      const float r0 = (s_fin[2 * tid] + s_fin[16 + 2 * tid]) + (s_fin[32 + 2 * tid] + s_fin[48 + 2 * tid]);
      const float r1 = (s_fin[2 * tid + 1] + s_fin[16 + 2 * tid + 1]) + (s_fin[32 + 2 * tid + 1] + s_fin[48 + 2 * tid + 1]);
      u32 o = pack_bf16(r0, r1);
      if (timeout) o = 0x7fc07fc0u;
      *(GAS u32*)((char*)p.out + (fb * 512 + fc * 16 + 2 * tid) * 2) = o;
    }
  }
}

extern "C" void kernel_launch(void* const* d_in, const int* in_sizes, int n_in, void* d_out, int out_size, void* d_ws, size_t ws_size,
                              hipStream_t stream) {
  (void)in_sizes; (void)n_in; (void)out_size; (void)ws_size;
  Params p;
  for (int b = 0; b < 2; ++b) {
    p.x[b] = (const char*)d_in[5 * b + 0]; p.k[b] = (const char*)d_in[5 * b + 1]; p.v[b] = (const char*)d_in[5 * b + 2];
    p.wq[b] = (const char*)d_in[5 * b + 3]; p.wo[b] = (const char*)d_in[5 * b + 4];
  }
  p.out = (unsigned short*)d_out; p.ws = (char*)d_ws;
  (void)hipFuncSetAttribute((const void*)concat_heads_attn_kernel, hipFuncAttributeMaxDynamicSharedMemorySize, LDS_BYTES);
  hipLaunchKernelGGL(concat_heads_attn_kernel, dim3(NWG), dim3(NTHR), LDS_BYTES, stream, p);
}
```

```cpp
#define MEAS 0
#define MULT 1
#define MEAS_ALLFINAL 0
#include <hip/hip_runtime.h>
#include <hip/hip_bf16.h>
#include <stdint.h>


typedef unsigned u32;
typedef unsigned long long u64;
typedef u32 u32x4 __attribute__((ext_vector_type(4)));
typedef float f32x2 __attribute__((ext_vector_type(2)));
typedef float f32x4 __attribute__((ext_vector_type(4)));
typedef __bf16 bf16x8 __attribute__((ext_vector_type(8)));
typedef __bf16 bf16x2 __attribute__((ext_vector_type(2)));
#define GAS __attribute__((address_space(1)))
typedef GAS u64 gu64;

constexpr int D_MODEL = 512, HQ = 32, SK = 65536, DH = 128, WIN = 4096;
constexpr int NWG = 256, NTHR = 256;
constexpr int TILE_KEYS = 32, TILE_BYTES = TILE_KEYS * DH * 2;
constexpr int NPROD_A = 30, NPROD_B = 2;
constexpr float LOG2E = 1.4426950408889634f, SCALE = 0.08838834764831845f;
constexpr float THR = 8.0f / LOG2E;

constexpr size_t WS_GEN = 0;
constexpr size_t WS_Q = 4096;
constexpr size_t WS_H1D = WS_Q + (size_t)2 * 8 * 2 * 512 * 8;
constexpr size_t WS_H1M = WS_H1D + (size_t)2 * 8 * 16 * 32 * 32 * 8;
constexpr size_t WS_H2 = WS_H1M + (size_t)2 * 8 * 16 * 32 * 2 * 8;
constexpr size_t WS_END = WS_H2 + (size_t)2 * 32 * 128 * 16 * 8;

constexpr int LDS_BYTES = 84 * 1024;
constexpr int FEWN = 8;
constexpr int SLEEPN = 24, SLEEPL = 8;
constexpr u32 SPIN_CAP = 1u << 18;

struct Params {
  const char* x[2]; const char* wq[2]; const char* k[2]; const char* v[2]; char* ws; const char* wo[2];
  unsigned short* out;
};

__device__ __forceinline__ __amdgpu_buffer_rsrc_t mkrsrc(const void* p, int bytes) {
  return __builtin_amdgcn_make_buffer_rsrc((void*)p, 0, bytes, 0x00020000);
}
__device__ __forceinline__ u32x4 ld_nt(__amdgpu_buffer_rsrc_t r, u32 off) { return __builtin_amdgcn_raw_buffer_load_b128(r, off, 0, 2); }
__device__ __forceinline__ u32x4 ld_sc1(__amdgpu_buffer_rsrc_t r, u32 off) { return __builtin_amdgcn_raw_buffer_load_b128(r, off, 0, 16); }
__device__ __forceinline__ void st_sc1(__amdgpu_buffer_rsrc_t r, u32 off, u32x4 v) { __builtin_amdgcn_raw_buffer_store_b128(v, r, off, 0, 16); }
__device__ __forceinline__ float dot2(u32 a, u32 b, float c) {
  return __builtin_amdgcn_fdot2_f32_bf16(__builtin_bit_cast(bf16x2, a), __builtin_bit_cast(bf16x2, b), c, false);
}
__device__ __forceinline__ f32x4 mfma(u32x4 a, u32x4 b, f32x4 c) {
  return __builtin_amdgcn_mfma_f32_16x16x32_bf16(__builtin_bit_cast(bf16x8, a), __builtin_bit_cast(bf16x8, b), c, 0, 0, 0);
}
__device__ __forceinline__ u32 perm_lo(u32 hi, u32 lo) { return __builtin_amdgcn_perm(hi, lo, 0x05040100u); }
__device__ __forceinline__ u32 perm_hi(u32 hi, u32 lo) { return __builtin_amdgcn_perm(hi, lo, 0x07060302u); }
__device__ __forceinline__ u32x4 colfrag(const u32x4 (&r)[8], int t) {
  u32x4 a;
#pragma unroll
  for (int jj = 0; jj < 4; ++jj) a[jj] = (t & 1) ? perm_hi(r[2 * jj + 1][t >> 1], r[2 * jj][t >> 1]) : perm_lo(r[2 * jj + 1][t >> 1], r[2 * jj][t >> 1]);
  return a;
}
__device__ __forceinline__ u32 pack_bf16(float lo, float hi) { bf16x2 r = {(__bf16)lo, (__bf16)hi}; return __builtin_bit_cast(u32, r); }
__device__ __forceinline__ u32 cvtpk(float lo, float hi) { u32 r; asm("v_cvt_pk_bf16_f32 %0, %1, %2" : "=v"(r) : "v"(lo), "v"(hi)); return r; }
template <int CTRL> __device__ __forceinline__ float dpp(float x) {
  return __builtin_bit_cast(float, __builtin_amdgcn_mov_dpp(__builtin_bit_cast(int, x), CTRL, 0xf, 0xf, true));
}
constexpr int ROR2 = 0x122, ROR4 = 0x124, ROR8 = 0x128;
__device__ __forceinline__ float f(u32 x) { return __uint_as_float(x); }
__device__ __forceinline__ u32 u(float x) { return __float_as_uint(x); }
__device__ __forceinline__ float swap32_add(float a, float b) { auto s = __builtin_amdgcn_permlane32_swap(u(a), u(b), false, false); return f(s[0]) + f(s[1]); }
__device__ __forceinline__ float swap16_add(float a, float b) { auto s = __builtin_amdgcn_permlane16_swap(u(a), u(b), false, false); return f(s[0]) + f(s[1]); }
__device__ __forceinline__ float xrow_sum(float x) { x = swap16_add(x, x); return swap32_add(x, x); }
__device__ __forceinline__ float vmax0(float a, float b) { float r; asm("v_max_f32 %0, %1, %2" : "=v"(r) : "v"(a), "v"(b)); return r; }
__device__ __forceinline__ float vmax(float a, float b) { float r; asm("v_max_f32 %0, %1, %2\n\ts_nop 1" : "=v"(r) : "v"(a), "v"(b)); return r; }
__device__ __forceinline__ float xrow_max(float x) {
  { auto s = __builtin_amdgcn_permlane16_swap(u(x), u(x), false, false); x = vmax(f(s[0]), f(s[1])); }
  { auto s = __builtin_amdgcn_permlane32_swap(u(x), u(x), false, false); x = vmax(f(s[0]), f(s[1])); }
  return x;
}
#define DPP_MAX(NAME, CTRL) __device__ __forceinline__ float NAME(float x) { float r; \
  asm("s_nop 1\n\tv_max_f32_dpp %0, %1, %1 " CTRL " row_mask:0xf bank_mask:0xf\n\ts_nop 1" : "=&v"(r) : "v"(x)); return r; }
DPP_MAX(max_ror2, "row_ror:2") DPP_MAX(max_ror4, "row_ror:4") DPP_MAX(max_ror8, "row_ror:8")
__device__ __forceinline__ float ex2(float x) { return __builtin_amdgcn_exp2f(x); }
__device__ __forceinline__ void wg_barrier() { asm volatile("s_waitcnt lgkmcnt(0)\n\ts_barrier" ::: "memory"); }
__device__ __forceinline__ u32 enc24(float v, u32 tlo) { return ((u(v) + 0x80u) & 0xffffff00u) | tlo; }
__device__ __forceinline__ float dec24(u32 lo) { return f(lo & 0xffffff00u); }
__device__ __forceinline__ u32 bad40(const u32x4& g, u32 tlo, u32 thi) { return (((g[0] ^ tlo) | (g[2] ^ tlo)) & 0xffu) | (g[1] ^ thi) | (g[3] ^ thi); }
#define FENCE() __builtin_amdgcn_sched_barrier(0)


constexpr u32 WS_M = 8u << 20;
#define MREC(slot) do { if (lane == 0) __hip_atomic_store((gu64*)(p.ws + WS_M) + (slot) * 1024 + w * 4 + wave, ((u64)mtag << 32) | (u32)mt_, __ATOMIC_RELAXED, __HIP_MEMORY_SCOPE_AGENT); } while (0)
__global__ void __launch_bounds__(NTHR, 2)
concat_heads_attn_kernel(Params p) {
  extern __shared__ __attribute__((aligned(16))) char smem[];
  constexpr int ACC_LD = 132;
  float* s_qred = (float*)smem;
  u32* s_q = (u32*)(smem + 2048);
  float* s_ml = (float*)(smem + 3072);
  float* s_acc = (float*)(smem + 4096);
  u32* s_o = (u32*)(smem + 40960);
  float* s_fin = (float*)(smem + 41216);
  u32* s_flag = (u32*)(smem + 41984);

  const int tid = threadIdx.x, lane = tid & 63;
  const int wave = __builtin_amdgcn_readfirstlane(tid >> 6);
  const int w = blockIdx.x, g = w & 7, i = w >> 3;
  const int l15 = lane & 15, lq = lane >> 4;
  const u64 mt0 = __builtin_amdgcn_s_memrealtime();
  const int rb = i >> 4;

  u32x4 xw, wq[8];
  {
    const int row0 = 256 * ((i & 15) >> 3) + 64 * wave + 8 * (lane >> 3);
    const __amdgpu_buffer_rsrc_t rq = mkrsrc((rb ? p.wq[1] : p.wq[0]) + (size_t)(g * 512 + (i & 7) * 64) * 2, D_MODEL * HQ * DH * 2);
    const u32 off = (u32)row0 * (HQ * DH * 2) + (lane & 7) * 16;
#pragma unroll
    for (int n = 0; n < 8; ++n) wq[n] = ld_nt(rq, off + n * (HQ * DH * 2));
    xw = *(const GAS u32x4*)((rb ? p.x[1] : p.x[0]) + row0 * 2);
    FENCE();
  }
  u64 gen;
  asm volatile("s_load_dwordx2 %0, %1, 0x0" : "=s"(gen) : "s"(p.ws) : "memory");

  const int ab = i >= NPROD_A ? 1 : 0;
  const int pidx = ab ? i - NPROD_A : i;
  const int t0 = ab ? pidx * 64 : pidx * 68 + min(pidx, 8);
  const int nt = ab ? 64 : 68 + (pidx < 8 ? 1 : 0);
  const size_t kvoff = ((size_t)g * SK + (ab ? SK - WIN : 0) + (size_t)t0 * TILE_KEYS) * (DH * 2);
  const char* kp = (ab ? p.k[1] : p.k[0]) + kvoff;
  const char* vp = (ab ? p.v[1] : p.v[0]) + kvoff;
  const __amdgpu_buffer_rsrc_t rws = mkrsrc(p.ws, (int)WS_END);

  u32x4 K[2][8], V[2][8];
  const u32 koff0 = (4 * (l15 & 3) + (l15 >> 2)) * 256 + lq * 16, koff1 = koff0 + 4096;
  const u32 voff0 = lane * 16, voff1 = voff0 + 4096;
  auto issueK = [&](int r, int k, int half) {
    const int t = wave + 4 * k;
    const __amdgpu_buffer_rsrc_t rk = mkrsrc(kp + (size_t)t * TILE_BYTES, t < nt ? TILE_BYTES : 0);
    if (half != 1) {
#pragma unroll
      for (int j = 0; j < 4; ++j) K[r][j] = ld_nt(rk, koff0 + j * 64);
    }
    if (half != 0) {
#pragma unroll
      for (int j = 0; j < 4; ++j) K[r][4 + j] = ld_nt(rk, koff1 + j * 64);
    }
    FENCE();
  };
  auto issueV = [&](int r, int k) {
    const int t = wave + 4 * k;
    const __amdgpu_buffer_rsrc_t rv = mkrsrc(vp + (size_t)t * TILE_BYTES, t < nt ? TILE_BYTES : 0);
#pragma unroll
    for (int j = 0; j < 4; ++j) V[r][j] = ld_nt(rv, voff0 + j * 1024);
#pragma unroll
    for (int j = 0; j < 4; ++j) V[r][4 + j] = ld_nt(rv, voff1 + j * 1024);
    FENCE();
  };
  FENCE();
  asm volatile("s_waitcnt vmcnt(4)" ::: "memory");
  FENCE();
  issueK(0, 0, 0);

  {
    float a[8];
#pragma unroll
    for (int c = 0; c < 8; ++c) a[c] = 0.f;
#pragma unroll
    for (int n = 0; n < 8; n += 2)
#pragma unroll
      for (int c = 0; c < 4; ++c) {
        a[2 * c] = dot2(perm_lo(wq[n + 1][c], wq[n][c]), xw[n >> 1], a[2 * c]);
        a[2 * c + 1] = dot2(perm_hi(wq[n + 1][c], wq[n][c]), xw[n >> 1], a[2 * c + 1]);
      }
    float b[4];
#pragma unroll
    for (int c = 0; c < 4; ++c) b[c] = swap32_add(a[c], a[c + 4]);
    float y0 = swap16_add(b[0], b[2]), y1 = swap16_add(b[1], b[3]);
    y0 += dpp<ROR8>(y0); y1 += dpp<ROR8>(y1);
    if (l15 < 8) *(float2*)(s_qred + wave * 64 + 8 * l15 + 2 * lq) = make_float2(y0, y1);
  }
  wg_barrier();
  asm volatile("s_waitcnt lgkmcnt(0)" : "+s"(gen) :: "memory");
  const u64 tag = gen + 1;
  const u32 thi = (u32)(tag >> 8), tlo = (u32)tag & 0xffu;
  const u32 qtag = (u32)tag;
  const u32 mtag = qtag ^ 0x5a5a5a5au;
  { const u64 mt_ = mt0; MREC(0); }
  if (tid < 32) {
    const float2 a0 = *(const float2*)(s_qred + 2 * lane), a1 = *(const float2*)(s_qred + 64 + 2 * lane);
    const float2 a2 = *(const float2*)(s_qred + 128 + 2 * lane), a3 = *(const float2*)(s_qred + 192 + 2 * lane);
    st_sc1(rws, (u32)WS_Q + (((rb * 8 + g) * 2 + ((i & 15) >> 3)) * 512 + (i & 7) * 64 + 2 * lane) * 8,
           u32x4{u((a0.x + a1.x) + (a2.x + a3.x)), qtag, u((a0.y + a1.y) + (a2.y + a3.y)), qtag});
  }

  const u32 qoff = (u32)WS_Q + ((ab * 8 + g) * 2 * 512 + 2 * tid) * 8, qoff1 = qoff + 512 * 8;
  bool timeout = false, okw = false, have = false;
  u32x4 qa0 = {0u, 0u, 0u, 0u}, qa1 = qa0;
  const u32x4 srd_ws = {(u32)(uintptr_t)p.ws, (u32)((uintptr_t)p.ws >> 32) & 0xffffu, (u32)WS_END, 0x00020000u};
#define Q_POLL() do { \
    asm volatile("s_nop 4\n\tbuffer_load_dwordx4 %0, %2, %4, 0 offen sc1\n\tbuffer_load_dwordx4 %1, %3, %4, 0 offen sc1" \
                 : "+v"(qa0), "+v"(qa1) : "v"(qoff), "v"(qoff1), "s"(srd_ws) : "memory"); \
    FENCE(); } while (0)
#define Q_WAIT(N) asm volatile("s_waitcnt vmcnt(" #N ")" : "+v"(qa0), "+v"(qa1) :: "memory")
  auto q_extract = [&]() {
    const u32 bad = (qa0[1] ^ qtag) | (qa0[3] ^ qtag) | (qa1[1] ^ qtag) | (qa1[3] ^ qtag);
    if (__all(bad == 0)) {
      okw = true;
      s_q[tid] = pack_bf16(f(qa0[0]) + f(qa1[0]), f(qa0[2]) + f(qa1[2]));
    }
  };
  auto q_sync = [&](int it) {
    if (lane == 0) s_flag[(it & 1) * 4 + wave] = okw ? 1u : 0u;
    wg_barrier();
    const u32x4 fl = *(const u32x4*)(s_flag + (it & 1) * 4);
    have = __builtin_amdgcn_readfirstlane(fl[0] & fl[1] & fl[2] & fl[3]) != 0;
  };
  issueK(0, 0, 1); issueV(0, 0); issueK(1, 1, 2);
  asm volatile("s_barrier" ::: "memory");
  Q_POLL();
  asm volatile("s_barrier" ::: "memory");
  issueV(1, 1);
  Q_WAIT(8);
  q_extract();
  q_sync(0);
  for (u32 spins = 0; !have; ++spins) {
    if (spins > SPIN_CAP) { timeout = true; break; }
    __builtin_amdgcn_s_sleep(1);
    if (!okw) { Q_POLL(); Q_WAIT(0); q_extract(); }
    q_sync(spins + 1);
  }
  u32x4 Q[4];
#pragma unroll
  for (int uu = 0; uu < 4; ++uu) Q[uu] = *(const u32x4*)(s_q + (l15 & 3) * 64 + uu * 16 + lq * 4);

  const u64 mt3 = __builtin_amdgcn_s_memrealtime();
  const int kq = l15 >> 2;
  const bool hiT = kq >= 2, hiR = (kq & 1) != 0;
  float m = -INFINITY, mc = -INFINITY, mthr = -INFINITY, l = 0.f;
  f32x4 acc[8];
#pragma unroll
  for (int t = 0; t < 8; ++t) acc[t] = f32x4{0.f, 0.f, 0.f, 0.f};
  auto compute = [&](int r, int k) {
    if (wave + 4 * k < nt) {
      f32x4 s0 = {0.f, 0.f, 0.f, 0.f}, s1 = {0.f, 0.f, 0.f, 0.f};
#pragma unroll
      for (int uu = 0; uu < 4; ++uu) { s0 = mfma(K[r][uu], Q[uu], s0); s1 = mfma(K[r][4 + uu], Q[uu], s1); }
      FENCE();
      u32x4 A0 = colfrag(V[r], 0), A1 = colfrag(V[r], 1);
      asm volatile("" : "+v"(A0), "+v"(A1));
      FENCE();
      const float t0_ = hiT ? s1[0] : s0[0], t1_ = hiT ? s1[1] : s0[1], t2_ = hiT ? s1[2] : s0[2], t3_ = hiT ? s1[3] : s0[3];
      const float x0 = hiR ? t2_ : t0_, x1 = hiR ? t3_ : t1_;
      const u32 a = cvtpk(x0, x1);
      const f32x2 y = f32x2{f(a << 16), f(a & 0xffff0000u)} * SCALE;
      const u32 b = cvtpk(y[0], y[1]);
      const float z0 = f(b << 16), z1 = f(b & 0xffff0000u);
      if (__any(vmax0(z0, z1) > mthr)) {
        const float mn = vmax(m, xrow_max(vmax(z0, z1)));
        const float alpha = ex2((m - mn) * LOG2E);
        l *= alpha;
#pragma unroll
        for (int t = 0; t < 8; ++t) acc[t] *= alpha;
        m = mn; mc = mn * LOG2E; mthr = mn + THR;
      }
      const float p0 = ex2(fmaf(z0, LOG2E, -mc)), p1 = ex2(fmaf(z1, LOG2E, -mc));
      const u32 pp = pack_bf16(p0, p1);
      const u32x4 P = {kq == 0 ? pp : 0u, kq == 1 ? pp : 0u, kq == 2 ? pp : 0u, kq == 3 ? pp : 0u};
      l = dot2(pp, 0x3f803f80u, l);
      FENCE();
#pragma unroll
      for (int t = 0; t < 8; t += 2) {
        acc[t] = mfma(A0, P, acc[t]);
        FENCE();
        if (t + 2 < 8) A0 = colfrag(V[r], t + 2);
        FENCE();
        acc[t + 1] = mfma(A1, P, acc[t + 1]);
        FENCE();
        if (t + 3 < 8) A1 = colfrag(V[r], t + 3);
        FENCE();
      }
    }
    FENCE();
  };
#pragma nounroll
  for (int k = 0; k < 16; k += 2) {
    compute(0, k); issueK(0, k + 2, 2); issueV(0, k + 2);
    compute(1, k + 1); issueK(1, k + 3, 2); issueV(1, k + 3);
  }
  compute(0, 16);
  compute(1, 17);
  const u64 mt4 = __builtin_amdgcn_s_memrealtime();
  const int hh = (i & 15) >> 2, sl = i & 3;
  const int np = rb ? NPROD_B : NPROD_A;
  const int crole = (rb * 8 + g) * 16 + (i & 15);
  const int cpp = lane >> 1;
  const u32 om = (u32)WS_H1M + (crole * 32 + cpp) * 16;
  u32x4 wo[8];
  {
    const __amdgpu_buffer_rsrc_t ro = mkrsrc((rb ? p.wo[1] : p.wo[0]) + (size_t)((g * 4 + hh) * DH + 32 * sl) * (D_MODEL * 2), 32 * D_MODEL * 2);
#pragma unroll
    for (int n = 0; n < 8; ++n) {
      const int jj = 8 * lq + n;
      wo[n] = ld_nt(ro, (u32)(8 * (jj & 3) + (jj >> 2)) * (D_MODEL * 2) + (128 * wave + 8 * l15) * 2);
    }
    FENCE();
  }

  {
    const float M = max_ror8(max_ror4(m));
    const float fs = ex2((m - M) * LOG2E);
    l *= fs;
    l += dpp<ROR8>(l); l += dpp<ROR4>(l);
    l = xrow_sum(l);
    float* dst = s_acc + (wave * 16 + l15) * ACC_LD + 32 * lq;
#pragma unroll
    for (int t = 0; t < 8; ++t) *(f32x4*)(dst + 4 * t) = acc[t] * fs;
    if (lane < 4) *(float2*)(s_ml + (wave * 4 + lane) * 2) = make_float2(M * LOG2E, l);
  }
  wg_barrier();
  {
    const int h = wave;
    float mw[4], lw[4];
    float2 a[4][4];
#pragma unroll
    for (int ww = 0; ww < 4; ++ww) { const float2 t = *(const float2*)(s_ml + (ww * 4 + h) * 2); mw[ww] = t.x; lw[ww] = t.y; }
#pragma unroll
    for (int ww = 0; ww < 4; ++ww)
#pragma unroll
      for (int k = 0; k < 4; ++k) a[ww][k] = *(const float2*)(s_acc + (ww * 16 + 4 * k + h) * ACC_LD + 2 * lane);
    const float M = dec24(enc24(vmax0(vmax0(mw[0], mw[1]), vmax0(mw[2], mw[3])), 0));
    float o0 = 0.f, o1 = 0.f, L = 0.f;
#pragma unroll
    for (int ww = 0; ww < 4; ++ww) {
      const float fw = ex2(mw[ww] - M);
      o0 = fmaf(fw, (a[ww][0].x + a[ww][1].x) + (a[ww][2].x + a[ww][3].x), o0);
      o1 = fmaf(fw, (a[ww][0].y + a[ww][1].y) + (a[ww][2].y + a[ww][3].y), o1);
      L = fmaf(fw, lw[ww], L);
    }
    const int role = (ab * 8 + g) * 16 + h * 4 + lq;
    st_sc1(rws, (u32)WS_H1D + ((role * 32 + pidx) * 32 + 2 * l15) * 8, u32x4{enc24(o0, tlo), thi, enc24(o1, tlo), thi});
    if (l15 == 0) st_sc1(rws, (u32)WS_H1M + (role * 32 + pidx) * 16, u32x4{enc24(M, tlo), thi, enc24(L, tlo), thi});
  }
  { const u64 mt_ = mt4; MREC(1); } { const u64 mt_ = mt3; MREC(2); }
  if (w == NWG - 1 && wave == 3) {
    for (u32 sp = 0;; ++sp) {
      u32 bad = 0;
#pragma unroll
      for (int r = 0; r < 4; ++r) { const u32x4 e = ld_sc1(rws, (u32)WS_Q + (lane + 64 * r) * (64 * 8)); bad |= e[1] ^ qtag; }
      if (__all(bad == 0)) break;
      if (timeout || sp > SPIN_CAP) break;
      __builtin_amdgcn_s_sleep(1);
    }
    if (lane == 0) __hip_atomic_store((gu64*)(p.ws + WS_GEN), tag, __ATOMIC_RELAXED, __HIP_MEMORY_SCOPE_AGENT);
  }

  u32x4 ml = ld_sc1(rws, cpp < np ? om : 0x7ffffff0u);
  FENCE();
  u32x4 B[8];
#pragma unroll
  for (int t = 0; t < 8; ++t) B[t] = colfrag(wo, t);
  {
    asm volatile("" : "+v"(B[0]), "+v"(B[1]), "+v"(B[2]), "+v"(B[3]), "+v"(B[4]), "+v"(B[5]), "+v"(B[6]), "+v"(B[7]));
    const u32 od = (u32)WS_H1D + ((crole * 32 + cpp) * 32 + 8 * wave + 4 * (lane & 1)) * 8;
    bool dmiss = cpp < np, mlmiss = dmiss && bad40(ml, tlo, thi) != 0;
    u32x4 d0 = {0u, 0u, 0u, 0u}, d1 = d0;
    for (u32 spins = 0;; ++spins) {
      const int nm = __popcll(__ballot(mlmiss));
      if (spins) {
        if (timeout || spins > SPIN_CAP) { timeout = true; break; }
        if (nm > SLEEPN) __builtin_amdgcn_s_sleep(SLEEPL); else if (nm) __builtin_amdgcn_s_sleep(1);
      }
      const bool getd = dmiss && (!mlmiss || nm <= FEWN);
      if (mlmiss) ml = ld_sc1(rws, om);
      if (getd) { d0 = ld_sc1(rws, od); d1 = ld_sc1(rws, od + 16); }
      if (mlmiss) mlmiss = bad40(ml, tlo, thi) != 0;
      if (getd) dmiss = (bad40(d0, tlo, thi) | bad40(d1, tlo, thi)) != 0;
      if (__ballot(mlmiss | dmiss) == 0) break;
    }
    const float mp = cpp < np ? dec24(ml[0]) : -INFINITY;
    const float M = xrow_max(max_ror8(max_ror4(max_ror2(mp))));
    const float fw = ex2(mp - M);
    float L = fw * dec24(ml[2]);
    const float v0 = fw * dec24(d0[0]), v1 = fw * dec24(d0[2]), v2 = fw * dec24(d1[0]), v3 = fw * dec24(d1[2]);
    L += dpp<ROR2>(L); L += dpp<ROR4>(L); L += dpp<ROR8>(L); L = xrow_sum(L);
    const float y0 = swap32_add(v0, v2), y1 = swap32_add(v1, v3);
    float z = swap16_add(y0, y1);
    z += dpp<ROR2>(z); z += dpp<ROR4>(z); z += dpp<ROR8>(z);
    z *= __builtin_amdgcn_rcpf(L);
    if (l15 < 2) ((unsigned short*)s_o)[8 * wave + 4 * l15 + lq] = (unsigned short)(pack_bf16(z, 0.f) & 0xffffu);
  }
  wg_barrier();
  {
    const u32x4 op = *(const u32x4*)(s_o + 4 * lq);
    f32x4 d[8];
#pragma unroll
    for (int t = 0; t < 8; ++t) d[t] = mfma(op, B[t], f32x4{0.f, 0.f, 0.f, 0.f});
    const float y0 = lq == 0 ? d[0][0] : lq == 1 ? d[2][0] : lq == 2 ? d[4][0] : d[6][0];
    const float y1 = lq == 0 ? d[1][0] : lq == 1 ? d[3][0] : lq == 2 ? d[5][0] : d[7][0];
    const int col = 128 * wave + 8 * l15 + 2 * lq;
    const int prod = g * 16 + (i & 15);
    st_sc1(rws, (u32)WS_H2 + (((rb * 32 + (col >> 4)) * 128 + prod) * 16 + (col & 15)) * 8, u32x4{enc24(y0, tlo), thi, enc24(y1, tlo), thi});
  }

  if (w < 64) {
    const int fb = w >> 5, fc = w & 31;
    const int cc = tid & 7, pp = tid >> 3;
    const u32 o2 = (u32)WS_H2 + (((fb * 32 + fc) * 128 + pp) * 16 + 2 * cc) * 8;
    u32x4 d[4];
    bool miss[4] = {true, true, true, true};
#pragma unroll
    for (int k = 0; k < 4; ++k) d[k] = u32x4{0u, 0u, 0u, 0u};
    for (u32 spins = 0;; ++spins) {
#pragma unroll
      for (int k = 0; k < 4; ++k) if (miss[k]) d[k] = ld_sc1(rws, o2 + k * (32 * 128));
#pragma unroll
      for (int k = 0; k < 4; ++k) miss[k] = bad40(d[k], tlo, thi) != 0;
      const int nm = __popcll(__ballot(miss[0])) + __popcll(__ballot(miss[1])) + __popcll(__ballot(miss[2])) + __popcll(__ballot(miss[3]));
      if (nm == 0) break;
      if (timeout || spins > SPIN_CAP) { timeout = true; break; }
      if (nm > 4 * SLEEPN) __builtin_amdgcn_s_sleep(SLEEPL); else __builtin_amdgcn_s_sleep(1);
    }
    const float y0 = (dec24(d[0][0]) + dec24(d[1][0])) + (dec24(d[2][0]) + dec24(d[3][0]));
    const float y1 = (dec24(d[0][2]) + dec24(d[1][2])) + (dec24(d[2][2]) + dec24(d[3][2]));
    float z = swap32_add(y0, y1);
    z = swap16_add(z, z);
    z += dpp<ROR8>(z);
    if ((lane & 31) < 8) s_fin[wave * 16 + 2 * cc + (lane >> 5)] = z;
    wg_barrier();
    if (tid < 8) {
      const float r0 = (s_fin[2 * tid] + s_fin[16 + 2 * tid]) + (s_fin[32 + 2 * tid] + s_fin[48 + 2 * tid]);
      const float r1 = (s_fin[2 * tid + 1] + s_fin[16 + 2 * tid + 1]) + (s_fin[32 + 2 * tid + 1] + s_fin[48 + 2 * tid + 1]);
      u32 o = pack_bf16(r0, r1);
      if (timeout) o = 0x7fc07fc0u;
      *(GAS u32*)((char*)p.out + (fb * 512 + fc * 16 + 2 * tid) * 2) = o;
    }
    if (wave == 0 && MEAS_ALLFINAL >= (w == 0 ? 0 : 1) && (w == 0 || MEAS_ALLFINAL)) {
      const u32 tnow = (u32)__builtin_amdgcn_s_memrealtime();
      u32 mn0 = 0xffffffffu, mx4 = 0, mx3 = 0; const u32 ref = (u32)mt0 - 1000u;
      for (int sp = 0; sp < 4096; ++sp) {
        u32 bad = 0; mn0 = 0xffffffffu; mx4 = 0; mx3 = 0;
        for (int r = 0; r < 16; ++r) {
          const u64 a = __hip_atomic_load((gu64*)(p.ws + WS_M) + lane + 64 * r, __ATOMIC_RELAXED, __HIP_MEMORY_SCOPE_AGENT);
          const u64 b = __hip_atomic_load((gu64*)(p.ws + WS_M) + 1024 + lane + 64 * r, __ATOMIC_RELAXED, __HIP_MEMORY_SCOPE_AGENT);
          const u64 c = __hip_atomic_load((gu64*)(p.ws + WS_M) + 2048 + lane + 64 * r, __ATOMIC_RELAXED, __HIP_MEMORY_SCOPE_AGENT);
          bad |= ((u32)(a >> 32) ^ mtag) | ((u32)(b >> 32) ^ mtag) | ((u32)(c >> 32) ^ mtag);
          mn0 = min(mn0, (u32)a - ref); mx4 = max(mx4, (u32)b - ref); mx3 = max(mx3, (u32)c - ref);
        }
        if (__all(bad == 0)) break;
      }
      for (int o_ = 32; o_; o_ >>= 1) { mn0 = min(mn0, (u32)__shfl_xor((int)mn0, o_)); mx4 = max(mx4, (u32)__shfl_xor((int)mx4, o_)); mx3 = max(mx3, (u32)__shfl_xor((int)mx3, o_)); }
      u32 iv = MEAS == 0 ? 0u : MEAS == 1 ? mx4 - mn0 : MEAS == 2 ? (tnow - ref) - mx4 : mx3 - mn0;
      if (iv > 20000u) iv = 0;
      asm volatile("" :: "v"(iv));
      const u64 until = __builtin_amdgcn_s_memrealtime() + (u64)iv * MULT;
      while (__builtin_amdgcn_s_memrealtime() < until) __builtin_amdgcn_s_sleep(4);
    }
  }
}

extern "C" void kernel_launch(void* const* d_in, const int* in_sizes, int n_in, void* d_out, int out_size, void* d_ws, size_t ws_size,
                              hipStream_t stream) {
  (void)in_sizes; (void)n_in; (void)out_size; (void)ws_size;
  Params p;
  for (int b = 0; b < 2; ++b) {
    p.x[b] = (const char*)d_in[5 * b + 0]; p.k[b] = (const char*)d_in[5 * b + 1]; p.v[b] = (const char*)d_in[5 * b + 2];
    p.wq[b] = (const char*)d_in[5 * b + 3]; p.wo[b] = (const char*)d_in[5 * b + 4];
  }
  p.out = (unsigned short*)d_out; p.ws = (char*)d_ws;
  (void)hipFuncSetAttribute((const void*)concat_heads_attn_kernel, hipFuncAttributeMaxDynamicSharedMemorySize, LDS_BYTES);
  hipLaunchKernelGGL(concat_heads_attn_kernel, dim3(NWG), dim3(NTHR), LDS_BYTES, stream, p);
}
```

```cpp
#include <hip/hip_runtime.h>
#include <hip/hip_bf16.h>
#include <stdint.h>

typedef unsigned u32;
typedef unsigned long long u64;
typedef u32 u32x4 __attribute__((ext_vector_type(4)));
typedef float f32x2 __attribute__((ext_vector_type(2)));
typedef float f32x4 __attribute__((ext_vector_type(4)));
typedef __bf16 bf16x8 __attribute__((ext_vector_type(8)));
typedef __bf16 bf16x2 __attribute__((ext_vector_type(2)));
#define GAS __attribute__((address_space(1)))
typedef GAS u64 gu64;

constexpr int D_MODEL = 512, HQ = 32, SK = 65536, DH = 128, WIN = 4096;
constexpr int NWG = 256, NTHR = 256;
constexpr int TILE_KEYS = 32, TILE_BYTES = TILE_KEYS * DH * 2;
constexpr int NPROD_A = 30, NPROD_B = 2;
constexpr float LOG2E = 1.4426950408889634f, SCALE = 0.08838834764831845f;
constexpr float THR = 8.0f / LOG2E;

constexpr size_t WS_GEN = 0;
constexpr size_t WS_Q = 4096;
constexpr size_t WS_H1D = WS_Q + (size_t)2 * 8 * 2 * 512 * 8;
constexpr size_t WS_H1M = WS_H1D + (size_t)2 * 8 * 16 * 32 * 32 * 8;
constexpr size_t WS_H2 = WS_H1M + (size_t)2 * 8 * 16 * 32 * 2 * 8;
constexpr size_t WS_END = WS_H2 + (size_t)2 * 32 * 128 * 16 * 8;

constexpr int LDS_BYTES = 42 * 1024;
constexpr int FEWN = 8;
constexpr int SLEEPN = 24, SLEEPL = 8;
constexpr u32 SPIN_CAP = 1u << 18;

struct Params {
  const char* x[2]; const char* wq[2]; const char* k[2]; const char* v[2]; char* ws; const char* wo[2];
  unsigned short* out;
};

__device__ __forceinline__ __amdgpu_buffer_rsrc_t mkrsrc(const void* p, int bytes) {
  return __builtin_amdgcn_make_buffer_rsrc((void*)p, 0, bytes, 0x00020000);
}
__device__ __forceinline__ u32x4 ld_nt(__amdgpu_buffer_rsrc_t r, u32 off) { return __builtin_amdgcn_raw_buffer_load_b128(r, off, 0, 2); }
__device__ __forceinline__ u32x4 ld_sc1(__amdgpu_buffer_rsrc_t r, u32 off) { return __builtin_amdgcn_raw_buffer_load_b128(r, off, 0, 16); }
__device__ __forceinline__ void st_sc1(__amdgpu_buffer_rsrc_t r, u32 off, u32x4 v) { __builtin_amdgcn_raw_buffer_store_b128(v, r, off, 0, 16); }
__device__ __forceinline__ float dot2(u32 a, u32 b, float c) {
  return __builtin_amdgcn_fdot2_f32_bf16(__builtin_bit_cast(bf16x2, a), __builtin_bit_cast(bf16x2, b), c, false);
}
__device__ __forceinline__ f32x4 mfma(u32x4 a, u32x4 b, f32x4 c) {
  return __builtin_amdgcn_mfma_f32_16x16x32_bf16(__builtin_bit_cast(bf16x8, a), __builtin_bit_cast(bf16x8, b), c, 0, 0, 0);
}
__device__ __forceinline__ u32 perm_lo(u32 hi, u32 lo) { return __builtin_amdgcn_perm(hi, lo, 0x05040100u); }
__device__ __forceinline__ u32 perm_hi(u32 hi, u32 lo) { return __builtin_amdgcn_perm(hi, lo, 0x07060302u); }
__device__ __forceinline__ u32x4 colfrag(const u32x4 (&r)[8], int t) {
  u32x4 a;
#pragma unroll
  for (int jj = 0; jj < 4; ++jj) a[jj] = (t & 1) ? perm_hi(r[2 * jj + 1][t >> 1], r[2 * jj][t >> 1]) : perm_lo(r[2 * jj + 1][t >> 1], r[2 * jj][t >> 1]);
  return a;
}
__device__ __forceinline__ u32 pack_bf16(float lo, float hi) { bf16x2 r = {(__bf16)lo, (__bf16)hi}; return __builtin_bit_cast(u32, r); }
__device__ __forceinline__ u32 cvtpk(float lo, float hi) { u32 r; asm("v_cvt_pk_bf16_f32 %0, %1, %2" : "=v"(r) : "v"(lo), "v"(hi)); return r; }
template <int CTRL> __device__ __forceinline__ float dpp(float x) {
  return __builtin_bit_cast(float, __builtin_amdgcn_mov_dpp(__builtin_bit_cast(int, x), CTRL, 0xf, 0xf, true));
}
constexpr int ROR2 = 0x122, ROR4 = 0x124, ROR8 = 0x128;
__device__ __forceinline__ float f(u32 x) { return __uint_as_float(x); }
__device__ __forceinline__ u32 u(float x) { return __float_as_uint(x); }
__device__ __forceinline__ float swap32_add(float a, float b) { auto s = __builtin_amdgcn_permlane32_swap(u(a), u(b), false, false); return f(s[0]) + f(s[1]); }
__device__ __forceinline__ float swap16_add(float a, float b) { auto s = __builtin_amdgcn_permlane16_swap(u(a), u(b), false, false); return f(s[0]) + f(s[1]); }
__device__ __forceinline__ float xrow_sum(float x) { x = swap16_add(x, x); return swap32_add(x, x); }
__device__ __forceinline__ float vmax0(float a, float b) { float r; asm("v_max_f32 %0, %1, %2" : "=v"(r) : "v"(a), "v"(b)); return r; }
__device__ __forceinline__ float vmax(float a, float b) { float r; asm("v_max_f32 %0, %1, %2\n\ts_nop 1" : "=v"(r) : "v"(a), "v"(b)); return r; }
__device__ __forceinline__ float xrow_max(float x) {
  { auto s = __builtin_amdgcn_permlane16_swap(u(x), u(x), false, false); x = vmax(f(s[0]), f(s[1])); }
  { auto s = __builtin_amdgcn_permlane32_swap(u(x), u(x), false, false); x = vmax(f(s[0]), f(s[1])); }
  return x;
}
#define DPP_MAX(NAME, CTRL) __device__ __forceinline__ float NAME(float x) { float r; \
  asm("s_nop 1\n\tv_max_f32_dpp %0, %1, %1 " CTRL " row_mask:0xf bank_mask:0xf\n\ts_nop 1" : "=&v"(r) : "v"(x)); return r; }
DPP_MAX(max_ror2, "row_ror:2") DPP_MAX(max_ror4, "row_ror:4") DPP_MAX(max_ror8, "row_ror:8")
__device__ __forceinline__ float ex2(float x) { return __builtin_amdgcn_exp2f(x); }
__device__ __forceinline__ void wg_barrier() { asm volatile("s_waitcnt lgkmcnt(0)\n\ts_barrier" ::: "memory"); }
__device__ __forceinline__ u32 enc24(float v, u32 tlo) { return ((u(v) + 0x80u) & 0xffffff00u) | tlo; }
__device__ __forceinline__ float dec24(u32 lo) { return f(lo & 0xffffff00u); }
__device__ __forceinline__ u32 bad40(const u32x4& g, u32 tlo, u32 thi) { return (((g[0] ^ tlo) | (g[2] ^ tlo)) & 0xffu) | (g[1] ^ thi) | (g[3] ^ thi); }
#define FENCE() __builtin_amdgcn_sched_barrier(0)

__global__ void __launch_bounds__(NTHR, 2)
concat_heads_attn_kernel(Params p) {
  extern __shared__ __attribute__((aligned(16))) char smem[];
  constexpr int ACC_LD = 132;
  float* s_qred = (float*)smem;
  u32* s_q = (u32*)(smem + 2048);
  float* s_ml = (float*)(smem + 3072);
  float* s_acc = (float*)(smem + 4096);
  u32* s_o = (u32*)(smem + 40960);
  float* s_fin = (float*)(smem + 41216);
  u32* s_flag = (u32*)(smem + 41984);

  const int tid = threadIdx.x, lane = tid & 63;
  const int wave = __builtin_amdgcn_readfirstlane(tid >> 6);
  const int w = blockIdx.x, g = w & 7, i = w >> 3;
  const int l15 = lane & 15, lq = lane >> 4;
  const int rb = i >> 4;

  u32x4 xw, wq[8];
  {
    const int row0 = 256 * ((i & 15) >> 3) + 64 * wave + 8 * (lane >> 3);
    const __amdgpu_buffer_rsrc_t rq = mkrsrc((rb ? p.wq[1] : p.wq[0]) + (size_t)(g * 512 + (i & 7) * 64) * 2, D_MODEL * HQ * DH * 2);
    const u32 off = (u32)row0 * (HQ * DH * 2) + (lane & 7) * 16;
#pragma unroll
    for (int n = 0; n < 8; ++n) wq[n] = ld_nt(rq, off + n * (HQ * DH * 2));
    xw = *(const GAS u32x4*)((rb ? p.x[1] : p.x[0]) + row0 * 2);
    FENCE();
  }
  u64 gen;
  asm volatile("s_load_dwordx2 %0, %1, 0x0" : "=s"(gen) : "s"(p.ws) : "memory");

  const int ab = i >= NPROD_A ? 1 : 0;
  const int pidx = ab ? i - NPROD_A : i;
  const int t0 = ab ? pidx * 64 : pidx * 68 + min(pidx, 8);
  const int nt = ab ? 64 : 68 + (pidx < 8 ? 1 : 0);
  const size_t kvoff = ((size_t)g * SK + (ab ? SK - WIN : 0) + (size_t)t0 * TILE_KEYS) * (DH * 2);
  const char* kp = (ab ? p.k[1] : p.k[0]) + kvoff;
  const char* vp = (ab ? p.v[1] : p.v[0]) + kvoff;
  const __amdgpu_buffer_rsrc_t rws = mkrsrc(p.ws, (int)WS_END);

  u32x4 K[2][8], V[2][8];
  const u32 koff0 = (4 * (l15 & 3) + (l15 >> 2)) * 256 + lq * 16, koff1 = koff0 + 4096;
  const u32 voff0 = lane * 16, voff1 = voff0 + 4096;
  auto issueK = [&](int r, int k, int half) {
    const int t = wave + 4 * k;
    const __amdgpu_buffer_rsrc_t rk = mkrsrc(kp + (size_t)t * TILE_BYTES, t < nt ? TILE_BYTES : 0);
    if (half != 1) {
#pragma unroll
      for (int j = 0; j < 4; ++j) K[r][j] = ld_nt(rk, koff0 + j * 64);
    }
    if (half != 0) {
#pragma unroll
      for (int j = 0; j < 4; ++j) K[r][4 + j] = ld_nt(rk, koff1 + j * 64);
    }
    FENCE();
  };
  auto issueV = [&](int r, int k) {
    const int t = wave + 4 * k;
    const __amdgpu_buffer_rsrc_t rv = mkrsrc(vp + (size_t)t * TILE_BYTES, t < nt ? TILE_BYTES : 0);
#pragma unroll
    for (int j = 0; j < 4; ++j) V[r][j] = ld_nt(rv, voff0 + j * 1024);
#pragma unroll
    for (int j = 0; j < 4; ++j) V[r][4 + j] = ld_nt(rv, voff1 + j * 1024);
    FENCE();
  };
  FENCE();
  asm volatile("s_waitcnt vmcnt(4)" ::: "memory");
  FENCE();
  issueK(0, 0, 0);

  {
    float a[8];
#pragma unroll
    for (int c = 0; c < 8; ++c) a[c] = 0.f;
#pragma unroll
    for (int n = 0; n < 8; n += 2)
#pragma unroll
      for (int c = 0; c < 4; ++c) {
        a[2 * c] = dot2(perm_lo(wq[n + 1][c], wq[n][c]), xw[n >> 1], a[2 * c]);
        a[2 * c + 1] = dot2(perm_hi(wq[n + 1][c], wq[n][c]), xw[n >> 1], a[2 * c + 1]);
      }
    float b[4];
#pragma unroll
    for (int c = 0; c < 4; ++c) b[c] = swap32_add(a[c], a[c + 4]);
    float y0 = swap16_add(b[0], b[2]), y1 = swap16_add(b[1], b[3]);
    y0 += dpp<ROR8>(y0); y1 += dpp<ROR8>(y1);
    if (l15 < 8) *(float2*)(s_qred + wave * 64 + 8 * l15 + 2 * lq) = make_float2(y0, y1);
  }
  wg_barrier();
  asm volatile("s_waitcnt lgkmcnt(0)" : "+s"(gen) :: "memory");
  const u64 tag = gen + 1;
  const u32 thi = (u32)(tag >> 8), tlo = (u32)tag & 0xffu;
  const u32 qtag = (u32)tag;
  if (tid < 32) {
    const float2 a0 = *(const float2*)(s_qred + 2 * lane), a1 = *(const float2*)(s_qred + 64 + 2 * lane);
    const float2 a2 = *(const float2*)(s_qred + 128 + 2 * lane), a3 = *(const float2*)(s_qred + 192 + 2 * lane);
    st_sc1(rws, (u32)WS_Q + (((rb * 8 + g) * 2 + ((i & 15) >> 3)) * 512 + (i & 7) * 64 + 2 * lane) * 8,
           u32x4{u((a0.x + a1.x) + (a2.x + a3.x)), qtag, u((a0.y + a1.y) + (a2.y + a3.y)), qtag});
  }

  const u32 qoff = (u32)WS_Q + ((ab * 8 + g) * 2 * 512 + 2 * tid) * 8, qoff1 = qoff + 512 * 8;
  bool timeout = false, okw = false, have = false;
  u32x4 qa0 = {0u, 0u, 0u, 0u}, qa1 = qa0;
  const u32x4 srd_ws = {(u32)(uintptr_t)p.ws, (u32)((uintptr_t)p.ws >> 32) & 0xffffu, (u32)WS_END, 0x00020000u};
#define Q_POLL() do { \
    asm volatile("s_nop 4\n\tbuffer_load_dwordx4 %0, %2, %4, 0 offen sc1\n\tbuffer_load_dwordx4 %1, %3, %4, 0 offen sc1" \
                 : "+v"(qa0), "+v"(qa1) : "v"(qoff), "v"(qoff1), "s"(srd_ws) : "memory"); \
    FENCE(); } while (0)
#define Q_WAIT(N) asm volatile("s_waitcnt vmcnt(" #N ")" : "+v"(qa0), "+v"(qa1) :: "memory")
  auto q_extract = [&]() {
    const u32 bad = (qa0[1] ^ qtag) | (qa0[3] ^ qtag) | (qa1[1] ^ qtag) | (qa1[3] ^ qtag);
    if (__all(bad == 0)) {
      okw = true;
      s_q[tid] = pack_bf16(f(qa0[0]) + f(qa1[0]), f(qa0[2]) + f(qa1[2]));
    }
  };
  auto q_sync = [&](int it) {
    if (lane == 0) s_flag[(it & 1) * 4 + wave] = okw ? 1u : 0u;
    wg_barrier();
    const u32x4 fl = *(const u32x4*)(s_flag + (it & 1) * 4);
    have = __builtin_amdgcn_readfirstlane(fl[0] & fl[1] & fl[2] & fl[3]) != 0;
  };
  issueK(0, 0, 1); issueV(0, 0); issueK(1, 1, 2);
  asm volatile("s_barrier" ::: "memory");
  Q_POLL();
  asm volatile("s_barrier" ::: "memory");
  issueV(1, 1);
  Q_WAIT(8);
  q_extract();
  q_sync(0);
  for (u32 spins = 0; !have; ++spins) {
    if (spins > SPIN_CAP) { timeout = true; break; }
    __builtin_amdgcn_s_sleep(1);
    if (!okw) { Q_POLL(); Q_WAIT(0); q_extract(); }
    q_sync(spins + 1);
  }
  u32x4 Q[4];
#pragma unroll
  for (int uu = 0; uu < 4; ++uu) Q[uu] = *(const u32x4*)(s_q + (l15 & 3) * 64 + uu * 16 + lq * 4);

  const int kq = l15 >> 2;
  const bool hiT = kq >= 2, hiR = (kq & 1) != 0;
  float m = -INFINITY, mc = -INFINITY, mthr = -INFINITY, l = 0.f;
  f32x4 acc[8];
#pragma unroll
  for (int t = 0; t < 8; ++t) acc[t] = f32x4{0.f, 0.f, 0.f, 0.f};
  auto compute = [&](int r, int k) {
    if (wave + 4 * k < nt) {
      f32x4 s0 = {0.f, 0.f, 0.f, 0.f}, s1 = {0.f, 0.f, 0.f, 0.f};
#pragma unroll
      for (int uu = 0; uu < 4; ++uu) { s0 = mfma(K[r][uu], Q[uu], s0); s1 = mfma(K[r][4 + uu], Q[uu], s1); }
      FENCE();
      u32x4 A0 = colfrag(V[r], 0), A1 = colfrag(V[r], 1);
      asm volatile("" : "+v"(A0), "+v"(A1));
      FENCE();
      const float t0_ = hiT ? s1[0] : s0[0], t1_ = hiT ? s1[1] : s0[1], t2_ = hiT ? s1[2] : s0[2], t3_ = hiT ? s1[3] : s0[3];
      const float x0 = hiR ? t2_ : t0_, x1 = hiR ? t3_ : t1_;
      const u32 a = cvtpk(x0, x1);
      const f32x2 y = f32x2{f(a << 16), f(a & 0xffff0000u)} * SCALE;
      const u32 b = cvtpk(y[0], y[1]);
      const float z0 = f(b << 16), z1 = f(b & 0xffff0000u);
      if (__any(vmax0(z0, z1) > mthr)) {
        const float mn = vmax(m, xrow_max(vmax(z0, z1)));
        const float alpha = ex2((m - mn) * LOG2E);
        l *= alpha;
#pragma unroll
        for (int t = 0; t < 8; ++t) acc[t] *= alpha;
        m = mn; mc = mn * LOG2E; mthr = mn + THR;
      }
      const float p0 = ex2(fmaf(z0, LOG2E, -mc)), p1 = ex2(fmaf(z1, LOG2E, -mc));
      const u32 pp = pack_bf16(p0, p1);
      const u32x4 P = {kq == 0 ? pp : 0u, kq == 1 ? pp : 0u, kq == 2 ? pp : 0u, kq == 3 ? pp : 0u};
      l = dot2(pp, 0x3f803f80u, l);
      FENCE();
#pragma unroll
      for (int t = 0; t < 8; t += 2) {
        acc[t] = mfma(A0, P, acc[t]);
        FENCE();
        if (t + 2 < 8) A0 = colfrag(V[r], t + 2);
        FENCE();
        acc[t + 1] = mfma(A1, P, acc[t + 1]);
        FENCE();
        if (t + 3 < 8) A1 = colfrag(V[r], t + 3);
        FENCE();
      }
    }
    FENCE();
  };
#pragma nounroll
  for (int k = 0; k < 16; k += 2) {
    compute(0, k); issueK(0, k + 2, 2); issueV(0, k + 2);
    compute(1, k + 1); issueK(1, k + 3, 2); issueV(1, k + 3);
  }
  compute(0, 16);
  compute(1, 17);
  const int hh = (i & 15) >> 2, sl = i & 3;
  const int np = rb ? NPROD_B : NPROD_A;
  const int crole = (rb * 8 + g) * 16 + (i & 15);
  const int cpp = lane >> 1;
  const u32 om = (u32)WS_H1M + (crole * 32 + cpp) * 16;
  u32x4 wo[8];
  {
    const __amdgpu_buffer_rsrc_t ro = mkrsrc((rb ? p.wo[1] : p.wo[0]) + (size_t)((g * 4 + hh) * DH + 32 * sl) * (D_MODEL * 2), 32 * D_MODEL * 2);
#pragma unroll
    for (int n = 0; n < 8; ++n) {
      const int jj = 8 * lq + n;
      wo[n] = ld_nt(ro, (u32)(8 * (jj & 3) + (jj >> 2)) * (D_MODEL * 2) + (128 * wave + 8 * l15) * 2);
    }
    FENCE();
  }

  {
    const float M = max_ror8(max_ror4(m));
    const float fs = ex2((m - M) * LOG2E);
    l *= fs;
    l += dpp<ROR8>(l); l += dpp<ROR4>(l);
    l = xrow_sum(l);
    float* dst = s_acc + (wave * 16 + l15) * ACC_LD + 32 * lq;
#pragma unroll
    for (int t = 0; t < 8; ++t) *(f32x4*)(dst + 4 * t) = acc[t] * fs;
    if (lane < 4) *(float2*)(s_ml + (wave * 4 + lane) * 2) = make_float2(M * LOG2E, l);
  }
  wg_barrier();
  {
    const int h = wave;
    float mw[4], lw[4];
    float2 a[4][4];
#pragma unroll
    for (int ww = 0; ww < 4; ++ww) { const float2 t = *(const float2*)(s_ml + (ww * 4 + h) * 2); mw[ww] = t.x; lw[ww] = t.y; }
#pragma unroll
    for (int ww = 0; ww < 4; ++ww)
#pragma unroll
      for (int k = 0; k < 4; ++k) a[ww][k] = *(const float2*)(s_acc + (ww * 16 + 4 * k + h) * ACC_LD + 2 * lane);
    const float M = dec24(enc24(vmax0(vmax0(mw[0], mw[1]), vmax0(mw[2], mw[3])), 0));
    float o0 = 0.f, o1 = 0.f, L = 0.f;
#pragma unroll
    for (int ww = 0; ww < 4; ++ww) {
      const float fw = ex2(mw[ww] - M);
      o0 = fmaf(fw, (a[ww][0].x + a[ww][1].x) + (a[ww][2].x + a[ww][3].x), o0);
      o1 = fmaf(fw, (a[ww][0].y + a[ww][1].y) + (a[ww][2].y + a[ww][3].y), o1);
      L = fmaf(fw, lw[ww], L);
    }
    const int role = (ab * 8 + g) * 16 + h * 4 + lq;
    st_sc1(rws, (u32)WS_H1D + ((role * 32 + pidx) * 32 + 2 * l15) * 8, u32x4{enc24(o0, tlo), thi, enc24(o1, tlo), thi});
    if (l15 == 0) st_sc1(rws, (u32)WS_H1M + (role * 32 + pidx) * 16, u32x4{enc24(M, tlo), thi, enc24(L, tlo), thi});
  }
  if (w == NWG - 1 && wave == 3) {
    for (u32 sp = 0;; ++sp) {
      u32 bad = 0;
#pragma unroll
      for (int r = 0; r < 4; ++r) { const u32x4 e = ld_sc1(rws, (u32)WS_Q + (lane + 64 * r) * (64 * 8)); bad |= e[1] ^ qtag; }
      if (__all(bad == 0)) break;
      if (timeout || sp > SPIN_CAP) break;
      __builtin_amdgcn_s_sleep(1);
    }
    if (lane == 0) __hip_atomic_store((gu64*)(p.ws + WS_GEN), tag, __ATOMIC_RELAXED, __HIP_MEMORY_SCOPE_AGENT);
  }

  u32x4 ml = ld_sc1(rws, cpp < np ? om : 0x7ffffff0u);
  FENCE();
  u32x4 B[8];
#pragma unroll
  for (int t = 0; t < 8; ++t) B[t] = colfrag(wo, t);
  {
    asm volatile("" : "+v"(B[0]), "+v"(B[1]), "+v"(B[2]), "+v"(B[3]), "+v"(B[4]), "+v"(B[5]), "+v"(B[6]), "+v"(B[7]));
    const u32 od = (u32)WS_H1D + ((crole * 32 + cpp) * 32 + 8 * wave + 4 * (lane & 1)) * 8;
    bool dmiss = cpp < np, mlmiss = dmiss && bad40(ml, tlo, thi) != 0;
    u32x4 d0 = {0u, 0u, 0u, 0u}, d1 = d0;
    for (u32 spins = 0;; ++spins) {
      const int nm = __popcll(__ballot(mlmiss));
      if (spins) {
        if (timeout || spins > SPIN_CAP) { timeout = true; break; }
        if (nm > SLEEPN) __builtin_amdgcn_s_sleep(SLEEPL); else if (nm) __builtin_amdgcn_s_sleep(1);
      }
      const bool getd = dmiss && (!mlmiss || nm <= FEWN);
      if (mlmiss) ml = ld_sc1(rws, om);
      if (getd) { d0 = ld_sc1(rws, od); d1 = ld_sc1(rws, od + 16); }
      if (mlmiss) mlmiss = bad40(ml, tlo, thi) != 0;
      if (getd) dmiss = (bad40(d0, tlo, thi) | bad40(d1, tlo, thi)) != 0;
      if (__ballot(mlmiss | dmiss) == 0) break;
    }
    const float mp = cpp < np ? dec24(ml[0]) : -INFINITY;
    const float M = xrow_max(max_ror8(max_ror4(max_ror2(mp))));
    const float fw = ex2(mp - M);
    float L = fw * dec24(ml[2]);
    const float v0 = fw * dec24(d0[0]), v1 = fw * dec24(d0[2]), v2 = fw * dec24(d1[0]), v3 = fw * dec24(d1[2]);
    L += dpp<ROR2>(L); L += dpp<ROR4>(L); L += dpp<ROR8>(L); L = xrow_sum(L);
    const float y0 = swap32_add(v0, v2), y1 = swap32_add(v1, v3);
    float z = swap16_add(y0, y1);
    z += dpp<ROR2>(z); z += dpp<ROR4>(z); z += dpp<ROR8>(z);
    z *= __builtin_amdgcn_rcpf(L);
    if (l15 < 2) ((unsigned short*)s_o)[8 * wave + 4 * l15 + lq] = (unsigned short)(pack_bf16(z, 0.f) & 0xffffu);
  }
  wg_barrier();
  {
    const u32x4 op = *(const u32x4*)(s_o + 4 * lq);
    f32x4 d[8];
#pragma unroll
    for (int t = 0; t < 8; ++t) d[t] = mfma(op, B[t], f32x4{0.f, 0.f, 0.f, 0.f});
    const float y0 = lq == 0 ? d[0][0] : lq == 1 ? d[2][0] : lq == 2 ? d[4][0] : d[6][0];
    const float y1 = lq == 0 ? d[1][0] : lq == 1 ? d[3][0] : lq == 2 ? d[5][0] : d[7][0];
    const int col = 128 * wave + 8 * l15 + 2 * lq;
    const int prod = g * 16 + (i & 15);
    st_sc1(rws, (u32)WS_H2 + (((rb * 32 + (col >> 4)) * 128 + prod) * 16 + (col & 15)) * 8, u32x4{enc24(y0, tlo), thi, enc24(y1, tlo), thi});
  }

  if (w < 64) {
    const int fb = w >> 5, fc = w & 31;
    const int cc = tid & 7, pp = tid >> 3;
    const u32 o2 = (u32)WS_H2 + (((fb * 32 + fc) * 128 + pp) * 16 + 2 * cc) * 8;
    u32x4 d[4];
    bool miss[4] = {true, true, true, true};
#pragma unroll
    for (int k = 0; k < 4; ++k) d[k] = u32x4{0u, 0u, 0u, 0u};
    for (u32 spins = 0;; ++spins) {
#pragma unroll
      for (int k = 0; k < 4; ++k) if (miss[k]) d[k] = ld_sc1(rws, o2 + k * (32 * 128));
#pragma unroll
      for (int k = 0; k < 4; ++k) miss[k] = bad40(d[k], tlo, thi) != 0;
      const int nm = __popcll(__ballot(miss[0])) + __popcll(__ballot(miss[1])) + __popcll(__ballot(miss[2])) + __popcll(__ballot(miss[3]));
      if (nm == 0) break;
      if (timeout || spins > SPIN_CAP) { timeout = true; break; }
      if (nm > 4 * SLEEPN) __builtin_amdgcn_s_sleep(SLEEPL); else __builtin_amdgcn_s_sleep(1);
    }
    const float y0 = (dec24(d[0][0]) + dec24(d[1][0])) + (dec24(d[2][0]) + dec24(d[3][0]));
    const float y1 = (dec24(d[0][2]) + dec24(d[1][2])) + (dec24(d[2][2]) + dec24(d[3][2]));
    float z = swap32_add(y0, y1);
    z = swap16_add(z, z);
    z += dpp<ROR8>(z);
    if ((lane & 31) < 8) s_fin[wave * 16 + 2 * cc + (lane >> 5)] = z;
    wg_barrier();
    if (tid < 8) {
      const float r0 = (s_fin[2 * tid] + s_fin[16 + 2 * tid]) + (s_fin[32 + 2 * tid] + s_fin[48 + 2 * tid]);
      const float r1 = (s_fin[2 * tid + 1] + s_fin[16 + 2 * tid + 1]) + (s_fin[32 + 2 * tid + 1] + s_fin[48 + 2 * tid + 1]);
      u32 o = pack_bf16(r0, r1);
      if (timeout) o = 0x7fc07fc0u;
      *(GAS u32*)((char*)p.out + (fb * 512 + fc * 16 + 2 * tid) * 2) = o;
    }
  }
}

extern "C" void kernel_launch(void* const* d_in, const int* in_sizes, int n_in, void* d_out, int out_size, void* d_ws, size_t ws_size,
                              hipStream_t stream) {
  (void)in_sizes; (void)n_in; (void)out_size; (void)ws_size;
  Params p;
  for (int b = 0; b < 2; ++b) {
    p.x[b] = (const char*)d_in[5 * b + 0]; p.k[b] = (const char*)d_in[5 * b + 1]; p.v[b] = (const char*)d_in[5 * b + 2];
    p.wq[b] = (const char*)d_in[5 * b + 3]; p.wo[b] = (const char*)d_in[5 * b + 4];
  }
  p.out = (unsigned short*)d_out; p.ws = (char*)d_ws;
  hipLaunchKernelGGL(concat_heads_attn_kernel, dim3(NWG), dim3(NTHR), LDS_BYTES, stream, p);
}
```
